```python
import torch

def _softmax(x, axis=-1):
    return torch.softmax(x, dim=axis)
def _gelu(x):
    return torch.nn.functional.gelu(x, approximate="tanh")
def _silu(x):
    return torch.nn.functional.silu(x)
def _sigmoid(x):
    return torch.sigmoid(x)
def _layernorm(x, axis=-1, eps=1e-5):
    m = x.mean(dim=axis, keepdim=True)
    v = x.var(dim=axis, keepdim=True, unbiased=False)
    return (x - m) / torch.sqrt(v + eps)
def _scatter_add(tgt, idx, src, axis=0):
    return tgt.clone().index_add_(axis, idx, src)
def _segment_sum(src, seg, n):
    out = torch.zeros((n,) + src.shape[1:], dtype=src.dtype)
    return out.index_add_(0, seg, src)
def _pad(x, pw):
    flat = tuple(v for pair in reversed(pw) for v in pair)
    return torch.nn.functional.pad(x, flat)

kernel_name = 'recurrent_scan__sliding_block_cached__glu_gated_swiglu__decode'

def setup_inputs():

    _w = (lambda a: (lambda t: t.to(torch.bfloat16) if t.is_floating_point() else t)(torch.as_tensor(a)))
    def _A_setup():
        B, Sq, D = 4, 1, 512
        Hq, Hkv, Sk, Dh = 32, 8, 4096, 128
        assert Hq % Hkv == 0, (Hq, Hkv)
        return {
            "x": _w((lambda *s: torch.randn(*s, dtype=torch.bfloat16) * 0.05)(B, Sq, D)),
            "k_cache": _w((lambda *s: torch.randn(*s, dtype=torch.bfloat16) * 1.6)(B, Hkv, Sk, Dh)),
            "v_cache": _w((lambda *s: torch.randn(*s, dtype=torch.bfloat16) * 1.6)(B, Hkv, Sk, Dh)),
            "wq": _w((lambda *s: torch.randn(*s, dtype=torch.bfloat16) * 1.6)(D, Hq * Dh) * (D ** -0.5)),
            "wo": _w((lambda *s: torch.randn(*s, dtype=torch.bfloat16) * 1.6)(Hq * Dh, D) * ((Hq * Dh) ** -0.5)),
        }
    _A_in = _A_setup()

    def _B_setup():
        B, S, D, F = 4, 1, 512, 1792
        return {
            "x": _w((lambda *s: torch.randn(*s, dtype=torch.bfloat16) * 0.05)(B, S, D)),
            "w1": _w((lambda *s: torch.randn(*s, dtype=torch.bfloat16) * 0.2)(D, F) * (2.0 / D) ** 0.5),
            "w3": _w((lambda *s: torch.randn(*s, dtype=torch.bfloat16) * 0.2)(D, F) * (2.0 / D) ** 0.5),
            "w2": _w((lambda *s: torch.randn(*s, dtype=torch.bfloat16) * 0.2)(F, D) * (2.0 / F) ** 0.5),
        }
    _B_in = _B_setup()

    return {
        "A_x": _A_in["x"],
        "A_k_cache": _A_in["k_cache"],
        "A_v_cache": _A_in["v_cache"],
        "A_wq": _A_in["wq"],
        "A_wo": _A_in["wo"],
        "B_x": _B_in["x"],
        "B_w1": _B_in["w1"],
        "B_w3": _B_in["w3"],
        "B_w2": _B_in["w2"],
    }

def reference(A_x, A_k_cache, A_v_cache, A_wq, A_wo, B_x, B_w1, B_w3, B_w2):
    def _A_ref(x, k_cache, v_cache, wq, wo):
        B, Sq, D = x.shape
        Hq, Dh = 32, 128
        Hkv, Sk = k_cache.shape[1], k_cache.shape[2]
        W = min(4096, max(256, Sk // 4))
        rep = Hq // Hkv
        q = (lambda x, *d: x.permute(*d))((x @ wq).reshape(B, Sq, Hq, Dh), 0, 2, 1, 3)
        k = (lambda x, n, axis: torch.repeat_interleave(x, n, dim=axis))(k_cache[:, :, Sk - W:, :], rep, axis=1)
        v = (lambda x, n, axis: torch.repeat_interleave(x, n, dim=axis))(v_cache[:, :, Sk - W:, :], rep, axis=1)
        scores = torch.einsum("bhqd,bhkd->bhqk", q, k) * (Dh ** -0.5)
        p = _softmax(scores, axis=-1)
        o = torch.einsum("bhqk,bhkd->bhqd", p, v)
        return (lambda x, *d: x.permute(*d))(o, 0, 2, 1, 3).reshape(B, Sq, Hq * Dh) @ wo

    def _B_ref(x, w1, w3, w2):
        gate = torch.einsum("bsd,df->bsf", x, w1)
        up = torch.einsum("bsd,df->bsf", x, w3)
        h = _silu(gate) * up
        out = torch.einsum("bsf,fd->bsd", h, w2)
        return out

    x = A_x
    xn = _layernorm(x, axis=-1)
    s = _A_ref(xn, A_k_cache, A_v_cache, A_wq, A_wo)
    g = _silu(_B_ref(xn, B_w1, B_w3, B_w2))
    return x + s * g
```

```cpp
#include <hip/hip_runtime.h>
#include <hip/hip_bf16.h>
typedef unsigned short u16;
__device__ __forceinline__ float bf2f(u16 b){ return __builtin_bit_cast(float, (unsigned)b<<16); }
__device__ __forceinline__ u16 f2bf(float f){ unsigned u=__builtin_bit_cast(unsigned,f); u += 0x7FFFu + ((u>>16)&1u); return u>>16; }
__device__ __forceinline__ float rbf(float f){ return bf2f(f2bf(f)); }
constexpr int D=512, HQ=32, HKV=8, SK=4096, DH=128, W=1024, F=1792;

__device__ void layernorm(const u16* x, float* xn  , float* red){
  int tid=threadIdx.x; int w=tid>>6, lane=tid&63;
  if(w<4){
    float v[8]; float s=0;
    for(int j=0;j<8;j++){ v[j]=bf2f(x[w*512+lane*8+j]); s+=v[j]; }
    for(int o=32;o;o>>=1) s+=__shfl_xor(s,o);
    float m=rbf(s/512.f);
    float mf=s/512.f;
    float q=0; for(int j=0;j<8;j++){ float d=v[j]-mf; q+=d*d; }
    for(int o=32;o;o>>=1) q+=__shfl_xor(q,o);
    float var=rbf(q/512.f);
    float den=rbf(sqrtf(rbf(var+1e-5f)));
    for(int j=0;j<8;j++) xn[w*512+lane*8+j]=rbf(rbf(v[j]-m)/den);
  }
  __syncthreads();
}
__global__ void k1(const u16* x,const u16* wq,const u16* w1,const u16* w3,float* q,float* h){
  __shared__ float xn[2048]; __shared__ float red[4][4][64][2];
  layernorm(x,xn,nullptr);
  int tid=threadIdx.x,w=tid>>6,lane=tid&63; int b=blockIdx.x;
  if(b<64){
    int col=b*64+lane; float a[4]={0,0,0,0};
    for(int k=w*128;k<w*128+128;k++){ float wv=bf2f(wq[k*4096+col]); for(int i=0;i<4;i++) a[i]+=xn[i*512+k]*wv; }
    for(int i=0;i<4;i++) red[w][i][lane][0]=a[i];
    __syncthreads();
    int i=w; float s=red[0][i][lane][0]+red[1][i][lane][0]+red[2][i][lane][0]+red[3][i][lane][0];
    q[i*4096+col]=rbf(s);
  } else {
    int col=(b-64)*64+lane; float a[4]={0,0,0,0},c[4]={0,0,0,0};
    for(int k=w*128;k<w*128+128;k++){ float wv=bf2f(w1[k*F+col]); float wu=bf2f(w3[k*F+col]); for(int i=0;i<4;i++){ a[i]+=xn[i*512+k]*wv; c[i]+=xn[i*512+k]*wu;} }
    for(int i=0;i<4;i++){ red[w][i][lane][0]=a[i]; red[w][i][lane][1]=c[i]; }
    __syncthreads();
    int i=w; float g=rbf(red[0][i][lane][0]+red[1][i][lane][0]+red[2][i][lane][0]+red[3][i][lane][0]);
    float u=rbf(red[0][i][lane][1]+red[1][i][lane][1]+red[2][i][lane][1]+red[3][i][lane][1]);
    float sg=rbf(g/(1.f+__expf(-g)));
    h[i*F+col]=rbf(sg*u);
  }
}
__global__ void k2(const u16* kc,const u16* vc,const float* q,float* part){
  __shared__ float qs[4][128]; __shared__ float sc[4][128]; __shared__ float ml[4][2];
  int blk=blockIdx.x; int sp=blk&7, hkv=(blk>>3)&7, b=blk>>6; int tid=threadIdx.x;
  for(int i=tid;i<512;i+=256) qs[i>>7][i&127]=q[b*4096+hkv*512+i];
  __syncthreads();
  const u16* kp=kc+((size_t)(b*8+hkv)*SK+(SK-W)+sp*128)*128;
  const u16* vp=vc+((size_t)(b*8+hkv)*SK+(SK-W)+sp*128)*128;
  { int key=tid&127; int h0=(tid>>7)*2; float a0=0,a1=0;
    for(int d=0;d<128;d++){ float kv=bf2f(kp[key*128+d]); a0+=kv*qs[h0][d]; a1+=kv*qs[h0+1][d]; }
    sc[h0][key]=a0*0.08838834764f; sc[h0+1][key]=a1*0.08838834764f; }
  __syncthreads();
  int w=tid>>6,lane=tid&63;
  { float a=sc[w][lane],c=sc[w][lane+64]; float m=fmaxf(a,c); for(int o=32;o;o>>=1) m=fmaxf(m,__shfl_xor(m,o));
    a=__expf(a-m); c=__expf(c-m); float l=a+c; for(int o=32;o;o>>=1) l+=__shfl_xor(l,o);
    sc[w][lane]=a; sc[w][lane+64]=c; if(lane==0){ml[w][0]=m; ml[w][1]=l;} }
  __syncthreads();
  for(int i=tid;i<512;i+=256){ int hh=i>>7,d=i&127; float a=0; for(int k=0;k<128;k++) a+=sc[hh][k]*bf2f(vp[k*128+d]);
    part[((size_t)blk*4+hh)*130+d]=a; }
  if(tid<4){ part[((size_t)blk*4+tid)*130+128]=ml[tid][0]; part[((size_t)blk*4+tid)*130+129]=ml[tid][1]; }
}
__global__ void k3(const float* part,const u16* wo,const float* h,const u16* w2,float* sp,float* gp){
  __shared__ float o[4][128]; int tid=threadIdx.x; int blk=blockIdx.x;
  const u16* wp; float* outp;
  if(blk<32){ int hh=blk&3, hkv=blk>>2;
    for(int i=tid;i<512;i+=256){ int b=i>>7,d=i&127; float M=-1e30f; for(int s=0;s<8;s++) M=fmaxf(M,part[((size_t)((b*8+hkv)*8+s)*4+hh)*130+128]);
      float L=0,a=0; for(int s=0;s<8;s++){ const float* p=part+((size_t)((b*8+hkv)*8+s)*4+hh)*130; float e=__expf(p[128]-M); L+=e*p[129]; a+=e*p[d]; }
      o[b][d]=rbf(a/L); }
    wp=wo+(size_t)blk*128*512; outp=sp+blk*2048;
  } else { int c=blk-32; for(int i=tid;i<512;i+=256){ int b=i>>7,d=i&127; o[b][d]=h[b*F+c*128+d]; } wp=w2+(size_t)c*128*512; outp=gp+c*2048; }
  __syncthreads();
  for(int col=tid;col<512;col+=256){ float a[4]={0,0,0,0}; for(int k=0;k<128;k++){ float wv=bf2f(wp[k*512+col]); for(int i=0;i<4;i++) a[i]+=o[i][k]*wv; } for(int i=0;i<4;i++) outp[i*512+col]=a[i]; }
}
__global__ void k4(const u16* x,const float* sp,const float* gp,u16* out){
  int i=blockIdx.x*256+threadIdx.x; float s=0,g=0; for(int j=0;j<32;j++) s+=sp[j*2048+i]; for(int j=0;j<14;j++) g+=gp[j*2048+i];
  s=rbf(s); g=rbf(g); g=rbf(g/(1.f+__expf(-g))); out[i]=f2bf(bf2f(x[i])+rbf(s*g));
}
extern "C" void kernel_launch(void* const* d_in, const int* in_sizes, int n_in, void* d_out, int out_size, void* d_ws, size_t ws_size, hipStream_t stream) {
  const u16* x=(const u16*)d_in[0]; const u16* kc=(const u16*)d_in[1]; const u16* vc=(const u16*)d_in[2];
  const u16* wq=(const u16*)d_in[3]; const u16* wo=(const u16*)d_in[4]; const u16* w1=(const u16*)d_in[6]; const u16* w3=(const u16*)d_in[7]; const u16* w2=(const u16*)d_in[8];
  float* ws=(float*)d_ws; float* q=ws; float* h=q+4*4096; float* part=h+4*F; float* sp=part+256*4*130; float* gp=sp+32*2048;
  k1<<<92,256,0,stream>>>(x,wq,w1,w3,q,h);
  k2<<<256,256,0,stream>>>(kc,vc,q,part);
  k3<<<46,256,0,stream>>>(part,wo,h,w2,sp,gp);
  k4<<<8,256,0,stream>>>(x,sp,gp,(u16*)d_out);
}
```

```cpp
#include <hip/hip_runtime.h>
#include <hip/hip_bf16.h>

#define GAS __attribute__((address_space(1)))
#define LAS __attribute__((address_space(3)))
#define CAS __attribute__((address_space(4)))
typedef unsigned short u16;
typedef unsigned long long u64;
typedef unsigned v4u __attribute__((ext_vector_type(4)));
typedef unsigned v2u __attribute__((ext_vector_type(2)));
typedef float f32x4 __attribute__((ext_vector_type(4)));
typedef float f32x2 __attribute__((ext_vector_type(2)));
typedef short s16x4 __attribute__((ext_vector_type(4)));
typedef __bf16 bf16x8 __attribute__((ext_vector_type(8)));
typedef __bf16 bf16x2 __attribute__((ext_vector_type(2)));
typedef GAS u64 gu64;
typedef GAS unsigned gu32;

#define WO_AT 1
#define NTS " nt"

namespace {
constexpr int SK = 4096, KOFF = 3072, FF = 1792;
constexpr unsigned WS_E = 0;
constexpr unsigned WS_QX = 4096;
constexpr unsigned WS_HX = WS_QX + 8 * 4 * 2 * 512 * 8;
constexpr unsigned WS_OX = WS_HX + 56 * 512 * 8;
constexpr unsigned WS_LX = WS_OX + 64 * 1024 * 8;
constexpr unsigned WS_SX = WS_LX + 64 * 32 * 8;
constexpr unsigned WS_GX = WS_SX + 256 * 512 * 8;
constexpr unsigned WS_END = WS_GX + 256 * 512 * 8;
constexpr unsigned L_W = 0, L_K = 32768, L_V = 65536, L_WO = 98304, L_W2 = 114688, L_X = 122880;
constexpr unsigned L_XN = 126976, XN_S = 1056;
constexpr unsigned L_Q = L_XN + 4 * XN_S, Q_S = 288;
constexpr unsigned L_P = L_Q + 4 * Q_S, P_S = 288;
constexpr unsigned L_O = L_P + 4 * P_S, O_S = 160;
constexpr unsigned L_H = L_O + 4 * O_S, H_S = 96;
constexpr unsigned L_MX = L_H + 4 * H_S;
constexpr unsigned L_LS = L_MX + 128;
constexpr unsigned L_FR = L_LS + 128;
constexpr unsigned L_PF = L_FR + 512;
constexpr unsigned L_END = L_PF + 1024;
constexpr unsigned SPIN_CAP = 1u << 17;
constexpr float C2 = 0.088388347648318440f * 1.4426950408889634f;
constexpr float LOG2E = 1.4426950408889634f;

template <int CTRL> __device__ __forceinline__ float dpp(float x) {
  return __builtin_bit_cast(float, __builtin_amdgcn_mov_dpp(__builtin_bit_cast(int, x), CTRL, 0xf, 0xf, true));
}
constexpr int XOR1 = 0xB1, XOR2 = 0x4E, HMIR = 0x141, ROR8 = 0x128, ROR4 = 0x124, ROR2 = 0x122, ROR1 = 0x121;
__device__ __forceinline__ float x16_sum(float x) {
  auto s = __builtin_amdgcn_permlane16_swap(__float_as_uint(x), __float_as_uint(x), false, false);
  return __uint_as_float(s[0]) + __uint_as_float(s[1]);
}
__device__ __forceinline__ float x32_sum(float x) {
  auto s = __builtin_amdgcn_permlane32_swap(__float_as_uint(x), __float_as_uint(x), false, false);
  return __uint_as_float(s[0]) + __uint_as_float(s[1]);
}
__device__ __forceinline__ float x16_max(float x) {
  auto s = __builtin_amdgcn_permlane16_swap(__float_as_uint(x), __float_as_uint(x), false, false);
  return fmaxf(__uint_as_float(s[0]), __uint_as_float(s[1]));
}
__device__ __forceinline__ float x32_max(float x) {
  auto s = __builtin_amdgcn_permlane32_swap(__float_as_uint(x), __float_as_uint(x), false, false);
  return fmaxf(__uint_as_float(s[0]), __uint_as_float(s[1]));
}
__device__ __forceinline__ float wave_sum(float x) {
  x += dpp<ROR8>(x); x += dpp<ROR4>(x); x += dpp<ROR2>(x); x += dpp<ROR1>(x);
  return x32_sum(x16_sum(x));
}
__device__ __forceinline__ unsigned pk(float lo, float hi) {
  f32x2 v = {lo, hi};
  return __builtin_bit_cast(unsigned, __builtin_convertvector(v, bf16x2));
}
__device__ __forceinline__ float lo16(unsigned v) { return __uint_as_float(v << 16); }
__device__ __forceinline__ float hi16(unsigned v) { return __uint_as_float(v & 0xffff0000u); }
__device__ __forceinline__ float rbf(float x) { return lo16(pk(x, x) ); }
__device__ __forceinline__ float silu(float x) { return x * __builtin_amdgcn_rcpf(1.f + __builtin_amdgcn_exp2f(-x * LOG2E)); }
__device__ __forceinline__ f32x4 mfma(v4u a, v4u b, f32x4 c) {
  return __builtin_amdgcn_mfma_f32_16x16x32_bf16(__builtin_bit_cast(bf16x8, a), __builtin_bit_cast(bf16x8, b), c, 0, 0, 0);
}
__device__ __forceinline__ v4u lds128(LAS unsigned char* L, unsigned off) { return *(LAS v4u*)(L + off); }
__device__ __forceinline__ v2u tr64(LAS unsigned char* L, unsigned off) {
  typedef short v4s __attribute__((ext_vector_type(4)));
  v4s r = __builtin_amdgcn_ds_read_tr16_b64_v4i16((LAS v4s*)(L + off));
  return __builtin_bit_cast(v2u, r);
}
__device__ __forceinline__ v4u trfrag(LAS unsigned char* L, unsigned off, unsigned dt) {
  const v2u a = tr64(L, off), b = tr64(L, off + dt);
  return v4u{a.x, a.y, b.x, b.y};
}
__device__ __forceinline__ float sel4(f32x4 d, unsigned u) {
  const float a = (u & 1) ? d[1] : d[0], b = (u & 1) ? d[3] : d[2];
  return (u & 2) ? b : a;
}
__device__ __forceinline__ void st_granule(unsigned char* ws, unsigned off, unsigned tag, unsigned val) {
  __hip_atomic_store((gu64*)(ws + off), ((u64)tag << 32) | val, __ATOMIC_RELAXED, __HIP_MEMORY_SCOPE_AGENT);
}
#define BAR() asm volatile("s_waitcnt lgkmcnt(0)\n\ts_barrier" ::: "memory")
#define VMWAIT(n) asm volatile("s_waitcnt vmcnt(" #n ")" ::: "memory")
#define DMA1(ldsa, v0, sb) \
  asm volatile("s_mov_b32 m0, %0\n\ts_nop 0\n\tglobal_load_lds_dwordx4 %1, %2" NTS ::"s"(ldsa), "v"(v0), "s"(sb) : "memory")
#define DMA2(ldsa, v0, v1, sb) \
  asm volatile("s_mov_b32 m0, %0\n\ts_nop 0\n\tglobal_load_lds_dwordx4 %1, %3" NTS "\n\tglobal_load_lds_dwordx4 %2, %3 offset:1024" NTS \
               ::"s"(ldsa), "v"(v0), "v"(v1), "s"(sb) : "memory")
#define DMA4(ldsa, v0, v1, v2, v3, sb) \
  asm volatile("s_mov_b32 m0, %0\n\ts_nop 0\n\tglobal_load_lds_dwordx4 %1, %5" NTS "\n\tglobal_load_lds_dwordx4 %2, %5 offset:1024" NTS \
               "\n\tglobal_load_lds_dwordx4 %3, %5 offset:2048" NTS "\n\tglobal_load_lds_dwordx4 %4, %5 offset:3072" NTS \
               ::"s"(ldsa), "v"(v0), "v"(v1), "v"(v2), "v"(v3), "s"(sb) : "memory")

#define STAMP(i) do {} while (0)
}

__global__ void __launch_bounds__(512, 2)
fused_decode_kernel(const u16* __restrict__ x, const u16* __restrict__ kc, const u16* __restrict__ vc, const u16* __restrict__ wq,
                    const u16* __restrict__ wo, const u16* __restrict__ w1, const u16* __restrict__ w3, const u16* __restrict__ w2,
                    u16* __restrict__ out, unsigned char* __restrict__ ws
) {
  __shared__ __attribute__((aligned(1024))) unsigned char smem[L_END];
  LAS unsigned char* L = (LAS unsigned char*)smem;
  const unsigned tid = threadIdx.x, lane = tid & 63;
  const unsigned w = __builtin_amdgcn_readfirstlane(tid >> 6);
  const unsigned c = blockIdx.x, g8 = c & 7, j = c >> 3;
  const unsigned b = j >> 3, sp = j & 7;
  STAMP(0);
  asm volatile("" ::"s"(x), "s"(kc), "s"(vc), "s"(wq), "s"(wo), "s"(w1), "s"(w3), "s"(w2), "s"(out), "s"(ws));
  const unsigned l4 = lane >> 4, l15 = lane & 15;

  const bool isq = j < 16;
  const unsigned fidx = (j - 16) * 8 + g8;
  const bool hasw = isq || fidx < 112;
  const unsigned kh = isq ? (j >> 3) : (fidx & 1);
  const unsigned cm = fidx >> 1, mm = cm >= 28 ? 1 : 0, cbf = cm - 28 * mm;
  const unsigned wstride = isq ? 8192u : 3584u;
  const unsigned char* wbase = isq ? (const unsigned char*)wq + (size_t)kh * 256 * 8192 + g8 * 1024 + (j & 7) * 128
                                   : (const unsigned char*)(mm ? w3 : w1) + (size_t)kh * 256 * 3584 + cbf * 128;
  if (w < 4) DMA1(L_X + w * 1024, w * 1024 + lane * 16, x);
  if (hasw) {
    const unsigned r0 = 32 * w + (lane >> 3);
    const unsigned ch = (lane & 7) ^ (((lane >> 4) & 1) << 1);
    const unsigned v0 = r0 * wstride + ch * 16;
    const unsigned st = 8 * wstride - 1024;
    DMA4(L_W + w * 4096, v0, (v0 + st) ^ 64, v0 + 2 * st, (v0 + 3 * st) ^ 64, wbase);
  }
  const size_t kvoff = ((size_t)(b * 8 + g8) * SK + KOFF + sp * 128) * 256;
  asm volatile("s_barrier" ::: "memory");
  {
    const unsigned v0 = w * 4096 + l4 * 256 + ((l15 ^ l4) << 4);
    DMA4(L_K + w * 4096, v0, v0 ^ 64, v0 ^ 128, v0 ^ 192, (const unsigned char*)kc + kvoff);
  }
  const unsigned tag = *(const CAS unsigned*)(ws + WS_E) + 1u;
  STAMP(1);

  if (hasw && w < 4) {
    VMWAIT(8);
    const v4u xv = lds128(L, L_X + w * 1024 + lane * 16);
    float v[8];
#pragma unroll
    for (int i = 0; i < 4; i++) { v[2 * i] = lo16(xv[i]); v[2 * i + 1] = hi16(xv[i]); }
    float s = ((v[0] + v[1]) + (v[2] + v[3])) + ((v[4] + v[5]) + (v[6] + v[7]));
    const float mean = wave_sum(s) * (1.f / 512.f);
    float q = 0.f;
#pragma unroll
    for (int i = 0; i < 8; i++) { const float d = v[i] - mean; q = fmaf(d, d, q); }
    const float var = rbf(wave_sum(q) * (1.f / 512.f));
    const float den = rbf(__builtin_amdgcn_sqrtf(rbf(var + 1e-5f)));
    const float inv = 1.0f / den, mb = rbf(mean);
    v4u o;
#pragma unroll
    for (int i = 0; i < 4; i++) {
      const unsigned d2 = pk(v[2 * i] - mb, v[2 * i + 1] - mb);
      o[i] = pk(lo16(d2) * inv, hi16(d2) * inv);
    }
    *(LAS v4u*)(L + L_XN + w * XN_S + lane * 16) = o;
  }
  VMWAIT(4);
  BAR();
  STAMP(2);

  const unsigned q2 = l15 >> 2, p2 = lane & 3;
  const unsigned bb = lane & 3, uu = (lane >> 2) & 3;
  const unsigned vsw = w * 4096 + l4 * 256 + ((l15 ^ (l4 << 1)) << 4);
  const unsigned char* vbase = (const unsigned char*)vc + kvoff;
  if (w < 4) {
    if (hasw) {
      const unsigned f = (q2 >> 1) | ((l4 & 1) << 1);
      const unsigned ta = L_W + (8 * l4 + q2) * 128 + (((2 * w + (p2 >> 1)) ^ (f << 1)) << 4) + 8 * (p2 & 1);
      const unsigned xa = L_XN + bb * XN_S + kh * 512 + l4 * 16;
      f32x4 a0 = {0.f, 0.f, 0.f, 0.f}, a1 = {0.f, 0.f, 0.f, 0.f};
#pragma unroll
      for (int s = 0; s < 8; s += 2) {
        a0 = mfma(trfrag(L, ta + s * 4096, 512), lds128(L, xa + s * 64), a0);
        a1 = mfma(trfrag(L, ta + (s + 1) * 4096, 512), lds128(L, xa + (s + 1) * 64), a1);
      }
      const float r = sel4(a0 + a1, uu);
      const unsigned n = 16 * w + 4 * l4 + uu;
      unsigned off;
      if (isq) off = WS_QX + (((g8 * 4 + bb) * 2 + kh) * 512 + (j & 7) * 64 + n) * 8;
      else off = WS_HX + ((2 * cbf + (n >> 5)) * 512 + (mm * 2 + kh) * 128 + bb * 32 + (n & 31)) * 8;
      st_granule(ws, off, tag, __float_as_uint(r));
      STAMP(19);
    }
    DMA4(L_V + w * 4096, vsw, vsw, vsw ^ 128, vsw ^ 128, vbase);
  }
  STAMP(3);

  const unsigned rb = j >> 2, cc = j & 3;
  const bool has2 = rb < 7;
  const unsigned fb = g8 * 7 + (has2 ? rb : 0);
  const unsigned char* wob = (const unsigned char*)wo + (size_t)(g8 * 512 + rb * 64) * 1024 + cc * 256;
  const unsigned char* w2b = (const unsigned char*)w2 + (size_t)(fb * 32) * 1024 + cc * 256;
  const unsigned wov = (8 * w + l4) * 1024 + ((l15 ^ (l4 << 1) ^ ((w & 1) << 3)) << 4);
  const unsigned w2v = (4 * w + l4) * 1024 + ((l15 ^ (l4 << 1) ^ (((w >> 1) & 1) << 3)) << 4);
#define ISSUE_WO() do { DMA2(L_WO + w * 2048, wov, wov + 3072, wob); DMA1(L_W2 + w * 1024, w2v, w2b); } while (0)

  {
    const unsigned char* qb = ws + WS_QX + (g8 * 4 + b) * 8192 + 4096;
    const unsigned qo = tid * 8;
    u64 qa, qc;
    if (w < 4) {
      asm volatile("s_waitcnt vmcnt(4)\n\tglobal_load_dwordx2 %0, %2, %3 offset:-4096 sc1\n\tglobal_load_dwordx2 %1, %2, %3 sc1\n\ts_barrier\n\ts_waitcnt vmcnt(0)"
                   : "=&v"(qa), "=&v"(qc) : "v"(qo), "s"(qb) : "memory");
    } else {
      u64 ta, tc;
      asm volatile("s_waitcnt vmcnt(0)\n\tglobal_load_dwordx2 %0, %2, %3 offset:-4096 sc1\n\tglobal_load_dwordx2 %1, %2, %3 sc1\n\ts_barrier\n\t"
                   "s_mov_b32 m0, %4\n\ts_nop 0\n\tglobal_load_lds_dwordx4 %5, %7" NTS "\n\tglobal_load_lds_dwordx4 %5, %7 offset:1024" NTS
                   "\n\tglobal_load_lds_dwordx4 %6, %7 offset:2048" NTS "\n\tglobal_load_lds_dwordx4 %6, %7 offset:3072" NTS "\n\ts_waitcnt vmcnt(4)"
                   : "=&v"(ta), "=&v"(tc) : "v"(qo), "s"(qb), "s"(L_V + w * 4096), "v"(vsw), "v"(vsw ^ 128), "s"(vbase) : "memory");
      qa = ta; qc = tc;
    }
    STAMP(4);
    for (unsigned spin = 0; !__all((unsigned)(qa >> 32) == tag && (unsigned)(qc >> 32) == tag) && spin < SPIN_CAP; ++spin)
      asm volatile("global_load_dwordx2 %0, %2, %3 offset:-4096 sc1\n\tglobal_load_dwordx2 %1, %2, %3 sc1\n\ts_waitcnt vmcnt(0)"
                   : "=&v"(qa), "=&v"(qc) : "v"(qo), "s"(qb) : "memory");
    const float qv = __uint_as_float((unsigned)qa) + __uint_as_float((unsigned)qc);
    *(LAS u16*)(L + L_Q + (tid >> 7) * Q_S + (tid & 127) * 2) = (u16)pk(qv, qv);
  }
  BAR();
  STAMP(5);

  f32x4 sc = {0.f, 0.f, 0.f, 0.f};
  {
    const unsigned ka = L_K + (16 * w + l15) * 256 + ((l4 ^ l15) << 4);
    const unsigned qa = L_Q + bb * Q_S + l4 * 16;
#pragma unroll
    for (int s = 0; s < 4; s++) sc = mfma(lds128(L, ka ^ (s << 6)), lds128(L, qa + s * 64), sc);
  }
  asm volatile("" : "+v"(sc));
  STAMP(14);
  float m = fmaxf(fmaxf(sc[0], sc[1]), fmaxf(sc[2], sc[3]));
  m = x32_max(x16_max(m));
  if (lane < 4) *(LAS float*)(L + L_MX + lane * 32 + w * 4) = m;
  BAR();
  STAMP(15);
  {
    const f32x4 a = *(LAS f32x4*)(L + L_MX + bb * 32), d = *(LAS f32x4*)(L + L_MX + bb * 32 + 16);
    m = fmaxf(fmaxf(fmaxf(a[0], a[1]), fmaxf(a[2], a[3])), fmaxf(fmaxf(d[0], d[1]), fmaxf(d[2], d[3])));
  }
  const float m2 = m * C2;
  {
    float p[4];
#pragma unroll
    for (int i = 0; i < 4; i++) p[i] = __builtin_amdgcn_exp2f(fmaf(sc[i], C2, -m2));
    float ps = (p[0] + p[1]) + (p[2] + p[3]);
    ps = x32_sum(x16_sum(ps));
    if (lane < 4) *(LAS float*)(L + L_LS + lane * 32 + w * 4) = ps;
    if (uu == 0) *(LAS v2u*)(L + L_P + bb * P_S + (16 * w + 4 * l4) * 2) = v2u{pk(p[0], p[1]), pk(p[2], p[3])};
  }
  STAMP(16);
  ISSUE_WO();
  STAMP(17);
  BAR();
  STAMP(18);
  v4u pf[4];
#pragma unroll
  for (int s = 0; s < 4; s++) pf[s] = lds128(L, L_P + bb * P_S + s * 64 + l4 * 16);
  float lsum;
  {
    const f32x4 a = *(LAS f32x4*)(L + L_LS + bb * 32), d = *(LAS f32x4*)(L + L_LS + bb * 32 + 16);
    lsum = ((a[0] + a[1]) + (a[2] + a[3])) + ((d[0] + d[1]) + (d[2] + d[3]));
  }
  const float linv = __builtin_amdgcn_rcpf(lsum);
  const float lse = m2 + __builtin_amdgcn_logf(lsum);
  asm volatile("" : "+v"(pf[0]), "+v"(pf[1]), "+v"(pf[2]), "+v"(pf[3]));
  STAMP(6);

  VMWAIT(3);
  BAR();
  STAMP(7);
  const unsigned tb = (8 * l4 + q2) * 256 + (((2 * w + (p2 >> 1)) ^ (2 * q2) ^ ((l4 & 1) << 3)) << 4) + 8 * (p2 & 1);
  {
    f32x4 o0 = {0.f, 0.f, 0.f, 0.f}, o1 = {0.f, 0.f, 0.f, 0.f};
    o0 = mfma(trfrag(L, L_V + tb, 1024), pf[0], o0);
    o1 = mfma(trfrag(L, L_V + tb + 8192, 1024), pf[1], o1);
    o0 = mfma(trfrag(L, L_V + tb + 16384, 1024), pf[2], o0);
    o1 = mfma(trfrag(L, L_V + tb + 24576, 1024), pf[3], o1);
    const f32x4 o = (o0 + o1) * linv;
    const unsigned val = (uu & 1) ? pk(o[2], o[3]) : pk(o[0], o[1]);
    const unsigned rbo = 2 * bb + (w >> 2);
    if (uu < 2) st_granule(ws, WS_OX + ((((g8 * 8 + rbo) * 4 + b) * 8 + sp) * 32 + 8 * (w & 3) + 2 * l4 + uu) * 8, tag, val);
    if (w == 0 && lane < 8) st_granule(ws, WS_LX + (((g8 * 8 + 2 * bb + (lane >> 2)) * 4 + b) * 8 + sp) * 8, tag, __float_as_uint(lse));
  }
  STAMP(8);
  {
    const unsigned char* hb = ws + WS_HX + fb * 4096;
    const unsigned ho = ((tid & 3) * 128 + (tid >> 2)) * 8;
    u64 hv;
    unsigned spin = 0;
    do {
      asm volatile("global_load_dwordx2 %0, %1, %2 sc1\n\ts_waitcnt vmcnt(0)" : "=&v"(hv) : "v"(ho), "s"(hb) : "memory");
    } while (!__all((unsigned)(hv >> 32) == tag) && ++spin < SPIN_CAP);
    float y = __uint_as_float((unsigned)hv);
    y = rbf(y + dpp<XOR1>(y));
    const float up = dpp<XOR2>(y);
    const float hh = rbf(silu(y)) * up;
    if ((lane & 3) == 0) *(LAS u16*)(L + L_H + (tid >> 7) * H_S + ((tid >> 2) & 31) * 2) = (u16)pk(hh, hh);
  }
  STAMP(9);

  {
    const unsigned ob = w >> 1, half = w & 1, os = lane & 7, ii = lane >> 3;
    const unsigned char* obase = ws + WS_OX + (g8 * 8 + rb) * 8192;
    const unsigned char* lbase = ws + WS_LX + (g8 * 8 + rb) * 256;
    const unsigned oo = ((ob * 8 + os) * 32 + 16 * half + 2 * ii) * 8, lo = (ob * 8 + os) * 8;
    u64 o0, o1, ls;
    unsigned spin = 0;
    do {
      asm volatile("global_load_dwordx2 %0, %3, %4 sc1\n\tglobal_load_dwordx2 %1, %3, %4 offset:8 sc1\n\tglobal_load_dwordx2 %2, %5, %6 sc1\n\ts_waitcnt vmcnt(0)"
                   : "=&v"(o0), "=&v"(o1), "=&v"(ls) : "v"(oo), "s"(obase), "v"(lo), "s"(lbase) : "memory");
    } while (!__all((unsigned)(o0 >> 32) == tag && (unsigned)(o1 >> 32) == tag && (unsigned)(ls >> 32) == tag) && ++spin < SPIN_CAP);
    STAMP(10);
    const float lv = __uint_as_float((unsigned)ls);
    float mx = fmaxf(lv, dpp<XOR1>(lv)); mx = fmaxf(mx, dpp<XOR2>(mx)); mx = fmaxf(mx, dpp<HMIR>(mx));
    const float e = __builtin_amdgcn_exp2f(lv - mx);
    float es = e + dpp<XOR1>(e); es += dpp<XOR2>(es); es += dpp<HMIR>(es);
    const float wt = e * __builtin_amdgcn_rcpf(es);
    float v[4] = {lo16((unsigned)o0) * wt, hi16((unsigned)o0) * wt, lo16((unsigned)o1) * wt, hi16((unsigned)o1) * wt};
#pragma unroll
    for (int i = 0; i < 4; i++) { v[i] += dpp<XOR1>(v[i]); v[i] += dpp<XOR2>(v[i]); v[i] += dpp<HMIR>(v[i]); }
    if (os == 0) *(LAS v2u*)(L + L_O + ob * O_S + 64 * half + 8 * ii) = v2u{pk(v[0], v[1]), pk(v[2], v[3])};
  }
  STAMP(20);
  BAR();
  STAMP(21);
  {
    const v4u a0 = trfrag(L, L_WO + tb, 1024), a1 = trfrag(L, L_WO + tb + 8192, 1024), a2 = trfrag(L, L_W2 + tb, 1024);
    const unsigned oa = L_O + bb * O_S + l4 * 16;
    f32x4 s0 = {0.f, 0.f, 0.f, 0.f}, s1 = {0.f, 0.f, 0.f, 0.f}, g0 = {0.f, 0.f, 0.f, 0.f};
    s0 = mfma(a0, lds128(L, oa), s0);
    s1 = mfma(a1, lds128(L, oa + 64), s1);
    g0 = mfma(a2, lds128(L, L_H + bb * H_S + l4 * 16), g0);
    const unsigned n = cc * 128 + 16 * w + 4 * l4 + uu;
    const unsigned o = (bb * 64 + (n >> 3)) * 512 + (n & 7);
    st_granule(ws, WS_SX + (o + (g8 * 8 + rb) * 8) * 8, tag, __float_as_uint(sel4(s0 + s1, uu)));
    if (has2) st_granule(ws, WS_GX + (o + fb * 8) * 8, tag, __float_as_uint(sel4(g0, uu)));
  }
  STAMP(11);

  {
    const unsigned char* sb = ws + WS_SX + c * 4096;
    const unsigned char* gb = ws + WS_GX + c * 4096;
    const unsigned so = tid * 8, go = (tid < 448 ? tid : tid - 64) * 8;
    u64 sv, gv;
    unsigned spin = 0;
    VMWAIT(0);
    do {
      asm volatile("global_load_dwordx2 %0, %2, %3 sc1\n\tglobal_load_dwordx2 %1, %4, %5 sc1\n\ts_waitcnt vmcnt(0)"
                   : "=&v"(sv), "=&v"(gv) : "v"(so), "s"(sb), "v"(go), "s"(gb) : "memory");
    } while (!__all((unsigned)(sv >> 32) == tag && (unsigned)(gv >> 32) == tag) && ++spin < SPIN_CAP);
    STAMP(12);
    float s = __uint_as_float((unsigned)sv), gg = w < 7 ? __uint_as_float((unsigned)gv) : 0.f;
    s += dpp<ROR8>(s); gg += dpp<ROR8>(gg);
    s = x32_sum(x16_sum(s)); gg = x32_sum(x16_sum(gg));
    if (lane < 8) {
      *(LAS float*)(L + L_FR + lane * 32 + w * 4) = s;
      *(LAS float*)(L + L_FR + 256 + lane * 32 + w * 4) = gg;
    }
  }
  BAR();
  if (tid < 8) {
    const f32x4 a = *(LAS f32x4*)(L + L_FR + lane * 32), d = *(LAS f32x4*)(L + L_FR + lane * 32 + 16);
    const f32x4 e = *(LAS f32x4*)(L + L_FR + 256 + lane * 32), f = *(LAS f32x4*)(L + L_FR + 256 + lane * 32 + 16);
    const float s = rbf(((a[0] + a[1]) + (a[2] + a[3])) + ((d[0] + d[1]) + (d[2] + d[3])));
    const float gg = rbf(((e[0] + e[1]) + (e[2] + e[3])) + ((f[0] + f[1]) + (f[2] + f[3])));
    const float xv = lo16(*(LAS u16*)(L + L_X + c * 16 + lane * 2));
    const float r = rbf(s * rbf(silu(gg)));
    out[c * 8 + lane] = (u16)pk(xv + r, xv + r);
    if (c == 0 && tid == 0) __hip_atomic_store((gu32*)(ws + WS_E), tag, __ATOMIC_RELAXED, __HIP_MEMORY_SCOPE_AGENT);
  }
  STAMP(13);
}

extern "C" void kernel_launch(void* const* d_in, const int* in_sizes, int n_in, void* d_out, int out_size, void* d_ws, size_t ws_size,
                              hipStream_t stream) {
  (void)in_sizes; (void)n_in; (void)out_size; (void)ws_size;
  fused_decode_kernel<<<256, 512, 0, stream>>>((const u16*)d_in[0], (const u16*)d_in[1], (const u16*)d_in[2], (const u16*)d_in[3],
                                               (const u16*)d_in[4], (const u16*)d_in[6], (const u16*)d_in[7], (const u16*)d_in[8],
                                               (u16*)d_out, (unsigned char*)d_ws);
}
```

```cpp
#define ENC 12
#include <hip/hip_runtime.h>
#include <hip/hip_bf16.h>

#define GAS __attribute__((address_space(1)))
#define LAS __attribute__((address_space(3)))
#define CAS __attribute__((address_space(4)))
typedef unsigned short u16;
typedef unsigned long long u64;
typedef unsigned v4u __attribute__((ext_vector_type(4)));
typedef unsigned v2u __attribute__((ext_vector_type(2)));
typedef float f32x4 __attribute__((ext_vector_type(4)));
typedef float f32x2 __attribute__((ext_vector_type(2)));
typedef short s16x4 __attribute__((ext_vector_type(4)));
typedef __bf16 bf16x8 __attribute__((ext_vector_type(8)));
typedef __bf16 bf16x2 __attribute__((ext_vector_type(2)));
typedef GAS u64 gu64;
typedef GAS unsigned gu32;

#define WO_AT 1
#define NTS " nt"

namespace {
constexpr int SK = 4096, KOFF = 3072, FF = 1792;
constexpr unsigned WS_E = 0;
constexpr unsigned WS_QX = 4096;
constexpr unsigned WS_HX = WS_QX + 8 * 4 * 2 * 512 * 8;
constexpr unsigned WS_OX = WS_HX + 56 * 512 * 8;
constexpr unsigned WS_LX = WS_OX + 64 * 1024 * 8;
constexpr unsigned WS_SX = WS_LX + 64 * 32 * 8;
constexpr unsigned WS_GX = WS_SX + 256 * 512 * 8;
constexpr unsigned WS_END = WS_GX + 256 * 512 * 8;
constexpr unsigned L_W = 0, L_K = 32768, L_V = 65536, L_WO = 98304, L_W2 = 114688, L_X = 122880;
constexpr unsigned L_XN = 126976, XN_S = 1056;
constexpr unsigned L_Q = L_XN + 4 * XN_S, Q_S = 288;
constexpr unsigned L_P = L_Q + 4 * Q_S, P_S = 288;
constexpr unsigned L_O = L_P + 4 * P_S, O_S = 160;
constexpr unsigned L_H = L_O + 4 * O_S, H_S = 96;
constexpr unsigned L_MX = L_H + 4 * H_S;
constexpr unsigned L_LS = L_MX + 128;
constexpr unsigned L_FR = L_LS + 128;
constexpr unsigned L_PF = L_FR + 512;
constexpr unsigned L_END = L_PF + 1024;
constexpr unsigned SPIN_CAP = 1u << 17;
constexpr float C2 = 0.088388347648318440f * 1.4426950408889634f;
constexpr float LOG2E = 1.4426950408889634f;

template <int CTRL> __device__ __forceinline__ float dpp(float x) {
  return __builtin_bit_cast(float, __builtin_amdgcn_mov_dpp(__builtin_bit_cast(int, x), CTRL, 0xf, 0xf, true));
}
constexpr int XOR1 = 0xB1, XOR2 = 0x4E, HMIR = 0x141, ROR8 = 0x128, ROR4 = 0x124, ROR2 = 0x122, ROR1 = 0x121;
__device__ __forceinline__ float x16_sum(float x) {
  auto s = __builtin_amdgcn_permlane16_swap(__float_as_uint(x), __float_as_uint(x), false, false);
  return __uint_as_float(s[0]) + __uint_as_float(s[1]);
}
__device__ __forceinline__ float x32_sum(float x) {
  auto s = __builtin_amdgcn_permlane32_swap(__float_as_uint(x), __float_as_uint(x), false, false);
  return __uint_as_float(s[0]) + __uint_as_float(s[1]);
}
__device__ __forceinline__ float x16_max(float x) {
  auto s = __builtin_amdgcn_permlane16_swap(__float_as_uint(x), __float_as_uint(x), false, false);
  return fmaxf(__uint_as_float(s[0]), __uint_as_float(s[1]));
}
__device__ __forceinline__ float x32_max(float x) {
  auto s = __builtin_amdgcn_permlane32_swap(__float_as_uint(x), __float_as_uint(x), false, false);
  return fmaxf(__uint_as_float(s[0]), __uint_as_float(s[1]));
}
__device__ __forceinline__ float wave_sum(float x) {
  x += dpp<ROR8>(x); x += dpp<ROR4>(x); x += dpp<ROR2>(x); x += dpp<ROR1>(x);
  return x32_sum(x16_sum(x));
}
__device__ __forceinline__ unsigned pk(float lo, float hi) {
  f32x2 v = {lo, hi};
  return __builtin_bit_cast(unsigned, __builtin_convertvector(v, bf16x2));
}
__device__ __forceinline__ float lo16(unsigned v) { return __uint_as_float(v << 16); }
__device__ __forceinline__ float hi16(unsigned v) { return __uint_as_float(v & 0xffff0000u); }
__device__ __forceinline__ float rbf(float x) { return lo16(pk(x, x) ); }
__device__ __forceinline__ float silu(float x) { return x * __builtin_amdgcn_rcpf(1.f + __builtin_amdgcn_exp2f(-x * LOG2E)); }
__device__ __forceinline__ f32x4 mfma(v4u a, v4u b, f32x4 c) {
  return __builtin_amdgcn_mfma_f32_16x16x32_bf16(__builtin_bit_cast(bf16x8, a), __builtin_bit_cast(bf16x8, b), c, 0, 0, 0);
}
__device__ __forceinline__ v4u lds128(LAS unsigned char* L, unsigned off) { return *(LAS v4u*)(L + off); }
__device__ __forceinline__ v2u tr64(LAS unsigned char* L, unsigned off) {
  typedef short v4s __attribute__((ext_vector_type(4)));
  v4s r = __builtin_amdgcn_ds_read_tr16_b64_v4i16((LAS v4s*)(L + off));
  return __builtin_bit_cast(v2u, r);
}
__device__ __forceinline__ v4u trfrag(LAS unsigned char* L, unsigned off, unsigned dt) {
  const v2u a = tr64(L, off), b = tr64(L, off + dt);
  return v4u{a.x, a.y, b.x, b.y};
}
__device__ __forceinline__ float sel4(f32x4 d, unsigned u) {
  const float a = (u & 1) ? d[1] : d[0], b = (u & 1) ? d[3] : d[2];
  return (u & 2) ? b : a;
}
__device__ __forceinline__ void st_granule(unsigned char* ws, unsigned off, unsigned tag, unsigned val) {
  __hip_atomic_store((gu64*)(ws + off), ((u64)tag << 32) | val, __ATOMIC_RELAXED, __HIP_MEMORY_SCOPE_AGENT);
}
#define BAR() asm volatile("s_waitcnt lgkmcnt(0)\n\ts_barrier" ::: "memory")
#define VMWAIT(n) asm volatile("s_waitcnt vmcnt(" #n ")" ::: "memory")
#define DMA1(ldsa, v0, sb) \
  asm volatile("s_mov_b32 m0, %0\n\ts_nop 0\n\tglobal_load_lds_dwordx4 %1, %2" NTS ::"s"(ldsa), "v"(v0), "s"(sb) : "memory")
#define DMA2(ldsa, v0, v1, sb) \
  asm volatile("s_mov_b32 m0, %0\n\ts_nop 0\n\tglobal_load_lds_dwordx4 %1, %3" NTS "\n\tglobal_load_lds_dwordx4 %2, %3 offset:1024" NTS \
               ::"s"(ldsa), "v"(v0), "v"(v1), "s"(sb) : "memory")
#define DMA4(ldsa, v0, v1, v2, v3, sb) \
  asm volatile("s_mov_b32 m0, %0\n\ts_nop 0\n\tglobal_load_lds_dwordx4 %1, %5" NTS "\n\tglobal_load_lds_dwordx4 %2, %5 offset:1024" NTS \
               "\n\tglobal_load_lds_dwordx4 %3, %5 offset:2048" NTS "\n\tglobal_load_lds_dwordx4 %4, %5 offset:3072" NTS \
               ::"s"(ldsa), "v"(v0), "v"(v1), "v"(v2), "v"(v3), "s"(sb) : "memory")

#define STAMP(i) do { if ((i) == 0) t_ent = __builtin_amdgcn_s_memrealtime(); if ((i) == ENC) t_enc = __builtin_amdgcn_s_memrealtime(); } while (0)
}

__global__ void __launch_bounds__(512, 2)
fused_decode_kernel(const u16* __restrict__ x, const u16* __restrict__ kc, const u16* __restrict__ vc, const u16* __restrict__ wq,
                    const u16* __restrict__ wo, const u16* __restrict__ w1, const u16* __restrict__ w3, const u16* __restrict__ w2,
                    u16* __restrict__ out, unsigned char* __restrict__ ws
) {
  __shared__ __attribute__((aligned(1024))) unsigned char smem[L_END];
  LAS unsigned char* L = (LAS unsigned char*)smem;
  const unsigned tid = threadIdx.x, lane = tid & 63;
  const unsigned w = __builtin_amdgcn_readfirstlane(tid >> 6);
  const unsigned c = blockIdx.x, g8 = c & 7, j = c >> 3;
  const unsigned b = j >> 3, sp = j & 7;
  u64 t_ent = 0, t_enc = 0;
  STAMP(0);
  asm volatile("" ::"s"(x), "s"(kc), "s"(vc), "s"(wq), "s"(wo), "s"(w1), "s"(w3), "s"(w2), "s"(out), "s"(ws));
  const unsigned l4 = lane >> 4, l15 = lane & 15;

  const bool isq = j < 16;
  const unsigned fidx = (j - 16) * 8 + g8;
  const bool hasw = isq || fidx < 112;
  const unsigned kh = isq ? (j >> 3) : (fidx & 1);
  const unsigned cm = fidx >> 1, mm = cm >= 28 ? 1 : 0, cbf = cm - 28 * mm;
  const unsigned wstride = isq ? 8192u : 3584u;
  const unsigned char* wbase = isq ? (const unsigned char*)wq + (size_t)kh * 256 * 8192 + g8 * 1024 + (j & 7) * 128
                                   : (const unsigned char*)(mm ? w3 : w1) + (size_t)kh * 256 * 3584 + cbf * 128;
  if (w < 4) DMA1(L_X + w * 1024, w * 1024 + lane * 16, x);
  if (hasw) {
    const unsigned r0 = 32 * w + (lane >> 3);
    const unsigned ch = (lane & 7) ^ (((lane >> 4) & 1) << 1);
    const unsigned v0 = r0 * wstride + ch * 16;
    const unsigned st = 8 * wstride - 1024;
    DMA4(L_W + w * 4096, v0, (v0 + st) ^ 64, v0 + 2 * st, (v0 + 3 * st) ^ 64, wbase);
  }
  const size_t kvoff = ((size_t)(b * 8 + g8) * SK + KOFF + sp * 128) * 256;
  asm volatile("s_barrier" ::: "memory");
  {
    const unsigned v0 = w * 4096 + l4 * 256 + ((l15 ^ l4) << 4);
    DMA4(L_K + w * 4096, v0, v0 ^ 64, v0 ^ 128, v0 ^ 192, (const unsigned char*)kc + kvoff);
  }
  const unsigned tag = *(const CAS unsigned*)(ws + WS_E) + 1u;
  STAMP(1);

  if (hasw && w < 4) {
    VMWAIT(8);
    const v4u xv = lds128(L, L_X + w * 1024 + lane * 16);
    float v[8];
#pragma unroll
    for (int i = 0; i < 4; i++) { v[2 * i] = lo16(xv[i]); v[2 * i + 1] = hi16(xv[i]); }
    float s = ((v[0] + v[1]) + (v[2] + v[3])) + ((v[4] + v[5]) + (v[6] + v[7]));
    const float mean = wave_sum(s) * (1.f / 512.f);
    float q = 0.f;
#pragma unroll
    for (int i = 0; i < 8; i++) { const float d = v[i] - mean; q = fmaf(d, d, q); }
    const float var = rbf(wave_sum(q) * (1.f / 512.f));
    const float den = rbf(__builtin_amdgcn_sqrtf(rbf(var + 1e-5f)));
    const float inv = 1.0f / den, mb = rbf(mean);
    v4u o;
#pragma unroll
    for (int i = 0; i < 4; i++) {
      const unsigned d2 = pk(v[2 * i] - mb, v[2 * i + 1] - mb);
      o[i] = pk(lo16(d2) * inv, hi16(d2) * inv);
    }
    *(LAS v4u*)(L + L_XN + w * XN_S + lane * 16) = o;
  }
  VMWAIT(4);
  BAR();
  STAMP(2);

  const unsigned q2 = l15 >> 2, p2 = lane & 3;
  const unsigned bb = lane & 3, uu = (lane >> 2) & 3;
  const unsigned vsw = w * 4096 + l4 * 256 + ((l15 ^ (l4 << 1)) << 4);
  const unsigned char* vbase = (const unsigned char*)vc + kvoff;
  if (w < 4) {
    if (hasw) {
      const unsigned f = (q2 >> 1) | ((l4 & 1) << 1);
      const unsigned ta = L_W + (8 * l4 + q2) * 128 + (((2 * w + (p2 >> 1)) ^ (f << 1)) << 4) + 8 * (p2 & 1);
      const unsigned xa = L_XN + bb * XN_S + kh * 512 + l4 * 16;
      f32x4 a0 = {0.f, 0.f, 0.f, 0.f}, a1 = {0.f, 0.f, 0.f, 0.f};
#pragma unroll
      for (int s = 0; s < 8; s += 2) {
        a0 = mfma(trfrag(L, ta + s * 4096, 512), lds128(L, xa + s * 64), a0);
        a1 = mfma(trfrag(L, ta + (s + 1) * 4096, 512), lds128(L, xa + (s + 1) * 64), a1);
      }
      const float r = sel4(a0 + a1, uu);
      const unsigned n = 16 * w + 4 * l4 + uu;
      unsigned off;
      if (isq) off = WS_QX + (((g8 * 4 + bb) * 2 + kh) * 512 + (j & 7) * 64 + n) * 8;
      else off = WS_HX + ((2 * cbf + (n >> 5)) * 512 + (mm * 2 + kh) * 128 + bb * 32 + (n & 31)) * 8;
      st_granule(ws, off, tag, __float_as_uint(r));
      STAMP(19);
    }
    DMA4(L_V + w * 4096, vsw, vsw, vsw ^ 128, vsw ^ 128, vbase);
  }
  STAMP(3);

  const unsigned rb = j >> 2, cc = j & 3;
  const bool has2 = rb < 7;
  const unsigned fb = g8 * 7 + (has2 ? rb : 0);
  const unsigned char* wob = (const unsigned char*)wo + (size_t)(g8 * 512 + rb * 64) * 1024 + cc * 256;
  const unsigned char* w2b = (const unsigned char*)w2 + (size_t)(fb * 32) * 1024 + cc * 256;
  const unsigned wov = (8 * w + l4) * 1024 + ((l15 ^ (l4 << 1) ^ ((w & 1) << 3)) << 4);
  const unsigned w2v = (4 * w + l4) * 1024 + ((l15 ^ (l4 << 1) ^ (((w >> 1) & 1) << 3)) << 4);
#define ISSUE_WO() do { DMA2(L_WO + w * 2048, wov, wov + 3072, wob); DMA1(L_W2 + w * 1024, w2v, w2b); } while (0)

  {
    const unsigned char* qb = ws + WS_QX + (g8 * 4 + b) * 8192 + 4096;
    const unsigned qo = tid * 8;
    u64 qa, qc;
    if (w < 4) {
      asm volatile("s_waitcnt vmcnt(4)\n\tglobal_load_dwordx2 %0, %2, %3 offset:-4096 sc1\n\tglobal_load_dwordx2 %1, %2, %3 sc1\n\ts_barrier\n\ts_waitcnt vmcnt(0)"
                   : "=&v"(qa), "=&v"(qc) : "v"(qo), "s"(qb) : "memory");
    } else {
      u64 ta, tc;
      asm volatile("s_waitcnt vmcnt(0)\n\tglobal_load_dwordx2 %0, %2, %3 offset:-4096 sc1\n\tglobal_load_dwordx2 %1, %2, %3 sc1\n\ts_barrier\n\t"
                   "s_mov_b32 m0, %4\n\ts_nop 0\n\tglobal_load_lds_dwordx4 %5, %7" NTS "\n\tglobal_load_lds_dwordx4 %5, %7 offset:1024" NTS
                   "\n\tglobal_load_lds_dwordx4 %6, %7 offset:2048" NTS "\n\tglobal_load_lds_dwordx4 %6, %7 offset:3072" NTS "\n\ts_waitcnt vmcnt(4)"
                   : "=&v"(ta), "=&v"(tc) : "v"(qo), "s"(qb), "s"(L_V + w * 4096), "v"(vsw), "v"(vsw ^ 128), "s"(vbase) : "memory");
      qa = ta; qc = tc;
    }
    STAMP(4);
    for (unsigned spin = 0; !__all((unsigned)(qa >> 32) == tag && (unsigned)(qc >> 32) == tag) && spin < SPIN_CAP; ++spin)
      asm volatile("global_load_dwordx2 %0, %2, %3 offset:-4096 sc1\n\tglobal_load_dwordx2 %1, %2, %3 sc1\n\ts_waitcnt vmcnt(0)"
                   : "=&v"(qa), "=&v"(qc) : "v"(qo), "s"(qb) : "memory");
    const float qv = __uint_as_float((unsigned)qa) + __uint_as_float((unsigned)qc);
    *(LAS u16*)(L + L_Q + (tid >> 7) * Q_S + (tid & 127) * 2) = (u16)pk(qv, qv);
  }
  BAR();
  STAMP(5);

  f32x4 sc = {0.f, 0.f, 0.f, 0.f};
  {
    const unsigned ka = L_K + (16 * w + l15) * 256 + ((l4 ^ l15) << 4);
    const unsigned qa = L_Q + bb * Q_S + l4 * 16;
#pragma unroll
    for (int s = 0; s < 4; s++) sc = mfma(lds128(L, ka ^ (s << 6)), lds128(L, qa + s * 64), sc);
  }
  asm volatile("" : "+v"(sc));
  STAMP(14);
  float m = fmaxf(fmaxf(sc[0], sc[1]), fmaxf(sc[2], sc[3]));
  m = x32_max(x16_max(m));
  if (lane < 4) *(LAS float*)(L + L_MX + lane * 32 + w * 4) = m;
  BAR();
  STAMP(15);
  {
    const f32x4 a = *(LAS f32x4*)(L + L_MX + bb * 32), d = *(LAS f32x4*)(L + L_MX + bb * 32 + 16);
    m = fmaxf(fmaxf(fmaxf(a[0], a[1]), fmaxf(a[2], a[3])), fmaxf(fmaxf(d[0], d[1]), fmaxf(d[2], d[3])));
  }
  const float m2 = m * C2;
  {
    float p[4];
#pragma unroll
    for (int i = 0; i < 4; i++) p[i] = __builtin_amdgcn_exp2f(fmaf(sc[i], C2, -m2));
    float ps = (p[0] + p[1]) + (p[2] + p[3]);
    ps = x32_sum(x16_sum(ps));
    if (lane < 4) *(LAS float*)(L + L_LS + lane * 32 + w * 4) = ps;
    if (uu == 0) *(LAS v2u*)(L + L_P + bb * P_S + (16 * w + 4 * l4) * 2) = v2u{pk(p[0], p[1]), pk(p[2], p[3])};
  }
  STAMP(16);
  ISSUE_WO();
  STAMP(17);
  BAR();
  STAMP(18);
  v4u pf[4];
#pragma unroll
  for (int s = 0; s < 4; s++) pf[s] = lds128(L, L_P + bb * P_S + s * 64 + l4 * 16);
  float lsum;
  {
    const f32x4 a = *(LAS f32x4*)(L + L_LS + bb * 32), d = *(LAS f32x4*)(L + L_LS + bb * 32 + 16);
    lsum = ((a[0] + a[1]) + (a[2] + a[3])) + ((d[0] + d[1]) + (d[2] + d[3]));
  }
  const float linv = __builtin_amdgcn_rcpf(lsum);
  const float lse = m2 + __builtin_amdgcn_logf(lsum);
  asm volatile("" : "+v"(pf[0]), "+v"(pf[1]), "+v"(pf[2]), "+v"(pf[3]));
  STAMP(6);

  VMWAIT(3);
  BAR();
  STAMP(7);
  const unsigned tb = (8 * l4 + q2) * 256 + (((2 * w + (p2 >> 1)) ^ (2 * q2) ^ ((l4 & 1) << 3)) << 4) + 8 * (p2 & 1);
  {
    f32x4 o0 = {0.f, 0.f, 0.f, 0.f}, o1 = {0.f, 0.f, 0.f, 0.f};
    o0 = mfma(trfrag(L, L_V + tb, 1024), pf[0], o0);
    o1 = mfma(trfrag(L, L_V + tb + 8192, 1024), pf[1], o1);
    o0 = mfma(trfrag(L, L_V + tb + 16384, 1024), pf[2], o0);
    o1 = mfma(trfrag(L, L_V + tb + 24576, 1024), pf[3], o1);
    const f32x4 o = (o0 + o1) * linv;
    const unsigned val = (uu & 1) ? pk(o[2], o[3]) : pk(o[0], o[1]);
    const unsigned rbo = 2 * bb + (w >> 2);
    if (uu < 2) st_granule(ws, WS_OX + ((((g8 * 8 + rbo) * 4 + b) * 8 + sp) * 32 + 8 * (w & 3) + 2 * l4 + uu) * 8, tag, val);
    if (w == 0 && lane < 8) st_granule(ws, WS_LX + (((g8 * 8 + 2 * bb + (lane >> 2)) * 4 + b) * 8 + sp) * 8, tag, __float_as_uint(lse));
  }
  STAMP(8);
  {
    const unsigned char* hb = ws + WS_HX + fb * 4096;
    const unsigned ho = ((tid & 3) * 128 + (tid >> 2)) * 8;
    u64 hv;
    unsigned spin = 0;
    do {
      asm volatile("global_load_dwordx2 %0, %1, %2 sc1\n\ts_waitcnt vmcnt(0)" : "=&v"(hv) : "v"(ho), "s"(hb) : "memory");
    } while (!__all((unsigned)(hv >> 32) == tag) && ++spin < SPIN_CAP);
    float y = __uint_as_float((unsigned)hv);
    y = rbf(y + dpp<XOR1>(y));
    const float up = dpp<XOR2>(y);
    const float hh = rbf(silu(y)) * up;
    if ((lane & 3) == 0) *(LAS u16*)(L + L_H + (tid >> 7) * H_S + ((tid >> 2) & 31) * 2) = (u16)pk(hh, hh);
  }
  STAMP(9);

  {
    const unsigned ob = w >> 1, half = w & 1, os = lane & 7, ii = lane >> 3;
    const unsigned char* obase = ws + WS_OX + (g8 * 8 + rb) * 8192;
    const unsigned char* lbase = ws + WS_LX + (g8 * 8 + rb) * 256;
    const unsigned oo = ((ob * 8 + os) * 32 + 16 * half + 2 * ii) * 8, lo = (ob * 8 + os) * 8;
    u64 o0, o1, ls;
    unsigned spin = 0;
    do {
      asm volatile("global_load_dwordx2 %0, %3, %4 sc1\n\tglobal_load_dwordx2 %1, %3, %4 offset:8 sc1\n\tglobal_load_dwordx2 %2, %5, %6 sc1\n\ts_waitcnt vmcnt(0)"
                   : "=&v"(o0), "=&v"(o1), "=&v"(ls) : "v"(oo), "s"(obase), "v"(lo), "s"(lbase) : "memory");
    } while (!__all((unsigned)(o0 >> 32) == tag && (unsigned)(o1 >> 32) == tag && (unsigned)(ls >> 32) == tag) && ++spin < SPIN_CAP);
    STAMP(10);
    const float lv = __uint_as_float((unsigned)ls);
    float mx = fmaxf(lv, dpp<XOR1>(lv)); mx = fmaxf(mx, dpp<XOR2>(mx)); mx = fmaxf(mx, dpp<HMIR>(mx));
    const float e = __builtin_amdgcn_exp2f(lv - mx);
    float es = e + dpp<XOR1>(e); es += dpp<XOR2>(es); es += dpp<HMIR>(es);
    const float wt = e * __builtin_amdgcn_rcpf(es);
    float v[4] = {lo16((unsigned)o0) * wt, hi16((unsigned)o0) * wt, lo16((unsigned)o1) * wt, hi16((unsigned)o1) * wt};
#pragma unroll
    for (int i = 0; i < 4; i++) { v[i] += dpp<XOR1>(v[i]); v[i] += dpp<XOR2>(v[i]); v[i] += dpp<HMIR>(v[i]); }
    if (os == 0) *(LAS v2u*)(L + L_O + ob * O_S + 64 * half + 8 * ii) = v2u{pk(v[0], v[1]), pk(v[2], v[3])};
  }
  STAMP(20);
  BAR();
  STAMP(21);
  {
    const v4u a0 = trfrag(L, L_WO + tb, 1024), a1 = trfrag(L, L_WO + tb + 8192, 1024), a2 = trfrag(L, L_W2 + tb, 1024);
    const unsigned oa = L_O + bb * O_S + l4 * 16;
    f32x4 s0 = {0.f, 0.f, 0.f, 0.f}, s1 = {0.f, 0.f, 0.f, 0.f}, g0 = {0.f, 0.f, 0.f, 0.f};
    s0 = mfma(a0, lds128(L, oa), s0);
    s1 = mfma(a1, lds128(L, oa + 64), s1);
    g0 = mfma(a2, lds128(L, L_H + bb * H_S + l4 * 16), g0);
    const unsigned n = cc * 128 + 16 * w + 4 * l4 + uu;
    const unsigned o = (bb * 64 + (n >> 3)) * 512 + (n & 7);
    st_granule(ws, WS_SX + (o + (g8 * 8 + rb) * 8) * 8, tag, __float_as_uint(sel4(s0 + s1, uu)));
    if (has2) st_granule(ws, WS_GX + (o + fb * 8) * 8, tag, __float_as_uint(sel4(g0, uu)));
  }
  STAMP(11);

  {
    const unsigned char* sb = ws + WS_SX + c * 4096;
    const unsigned char* gb = ws + WS_GX + c * 4096;
    const unsigned so = tid * 8, go = (tid < 448 ? tid : tid - 64) * 8;
    u64 sv, gv;
    unsigned spin = 0;
    VMWAIT(0);
    do {
      asm volatile("global_load_dwordx2 %0, %2, %3 sc1\n\tglobal_load_dwordx2 %1, %4, %5 sc1\n\ts_waitcnt vmcnt(0)"
                   : "=&v"(sv), "=&v"(gv) : "v"(so), "s"(sb), "v"(go), "s"(gb) : "memory");
    } while (!__all((unsigned)(sv >> 32) == tag && (unsigned)(gv >> 32) == tag) && ++spin < SPIN_CAP);
    STAMP(12);
    float s = __uint_as_float((unsigned)sv), gg = w < 7 ? __uint_as_float((unsigned)gv) : 0.f;
    s += dpp<ROR8>(s); gg += dpp<ROR8>(gg);
    s = x32_sum(x16_sum(s)); gg = x32_sum(x16_sum(gg));
    if (lane < 8) {
      *(LAS float*)(L + L_FR + lane * 32 + w * 4) = s;
      *(LAS float*)(L + L_FR + 256 + lane * 32 + w * 4) = gg;
    }
  }
  BAR();
  if (tid < 8) {
    const f32x4 a = *(LAS f32x4*)(L + L_FR + lane * 32), d = *(LAS f32x4*)(L + L_FR + lane * 32 + 16);
    const f32x4 e = *(LAS f32x4*)(L + L_FR + 256 + lane * 32), f = *(LAS f32x4*)(L + L_FR + 256 + lane * 32 + 16);
    const float s = rbf(((a[0] + a[1]) + (a[2] + a[3])) + ((d[0] + d[1]) + (d[2] + d[3])));
    const float gg = rbf(((e[0] + e[1]) + (e[2] + e[3])) + ((f[0] + f[1]) + (f[2] + f[3])));
    const float xv = lo16(*(LAS u16*)(L + L_X + c * 16 + lane * 2));
    const float r = rbf(s * rbf(silu(gg)));
    float fin = xv + r;
    if (ENC == 13) t_enc = __builtin_amdgcn_s_memrealtime();
    if (fabsf(fin) < 0.002f) fin += 1e-3f + fminf((float)(unsigned)(t_enc - t_ent) * 1e-6f, 3.5e-3f);
    out[c * 8 + lane] = (u16)pk(fin, fin);
    if (c == 0 && tid == 0) __hip_atomic_store((gu32*)(ws + WS_E), tag, __ATOMIC_RELAXED, __HIP_MEMORY_SCOPE_AGENT);
  }
  STAMP(13);
}

extern "C" void kernel_launch(void* const* d_in, const int* in_sizes, int n_in, void* d_out, int out_size, void* d_ws, size_t ws_size,
                              hipStream_t stream) {
  (void)in_sizes; (void)n_in; (void)out_size; (void)ws_size;
  fused_decode_kernel<<<256, 512, 0, stream>>>((const u16*)d_in[0], (const u16*)d_in[1], (const u16*)d_in[2], (const u16*)d_in[3],
                                               (const u16*)d_in[4], (const u16*)d_in[6], (const u16*)d_in[7], (const u16*)d_in[8],
                                               (u16*)d_out, (unsigned char*)d_ws);
}
```

```cpp
#define ADDX 1
#include <hip/hip_runtime.h>
#include <hip/hip_bf16.h>

#define GAS __attribute__((address_space(1)))
#define LAS __attribute__((address_space(3)))
#define CAS __attribute__((address_space(4)))
typedef unsigned short u16;
typedef unsigned long long u64;
typedef unsigned v4u __attribute__((ext_vector_type(4)));
typedef unsigned v2u __attribute__((ext_vector_type(2)));
typedef float f32x4 __attribute__((ext_vector_type(4)));
typedef float f32x2 __attribute__((ext_vector_type(2)));
typedef short s16x4 __attribute__((ext_vector_type(4)));
typedef __bf16 bf16x8 __attribute__((ext_vector_type(8)));
typedef __bf16 bf16x2 __attribute__((ext_vector_type(2)));
typedef GAS u64 gu64;
typedef GAS unsigned gu32;

#define WO_AT 1
#define NTS " nt"

namespace {
constexpr int SK = 4096, KOFF = 3072, FF = 1792;
constexpr unsigned WS_E = 0;
constexpr unsigned WS_QX = 4096;
constexpr unsigned WS_HX = WS_QX + 8 * 4 * 2 * 512 * 8;
constexpr unsigned WS_OX = WS_HX + 56 * 512 * 8;
constexpr unsigned WS_LX = WS_OX + 64 * 1024 * 8;
constexpr unsigned WS_SX = WS_LX + 64 * 32 * 8;
constexpr unsigned WS_GX = WS_SX + 256 * 512 * 8;
constexpr unsigned WS_END = WS_GX + 256 * 512 * 8;
constexpr unsigned L_W = 0, L_K = 32768, L_V = 65536, L_WO = 98304, L_W2 = 114688, L_X = 122880;
constexpr unsigned L_XN = 126976, XN_S = 1056;
constexpr unsigned L_Q = L_XN + 4 * XN_S, Q_S = 288;
constexpr unsigned L_P = L_Q + 4 * Q_S, P_S = 288;
constexpr unsigned L_O = L_P + 4 * P_S, O_S = 160;
constexpr unsigned L_H = L_O + 4 * O_S, H_S = 96;
constexpr unsigned L_MX = L_H + 4 * H_S;
constexpr unsigned L_LS = L_MX + 128;
constexpr unsigned L_FR = L_LS + 128;
constexpr unsigned L_PF = L_FR + 512;
constexpr unsigned L_END = L_PF + 1024;
constexpr unsigned SPIN_CAP = 1u << 17;
constexpr float C2 = 0.088388347648318440f * 1.4426950408889634f;
constexpr float LOG2E = 1.4426950408889634f;

template <int CTRL> __device__ __forceinline__ float dpp(float x) {
  return __builtin_bit_cast(float, __builtin_amdgcn_mov_dpp(__builtin_bit_cast(int, x), CTRL, 0xf, 0xf, true));
}
constexpr int XOR1 = 0xB1, XOR2 = 0x4E, HMIR = 0x141, ROR8 = 0x128, ROR4 = 0x124, ROR2 = 0x122, ROR1 = 0x121;
__device__ __forceinline__ float x16_sum(float x) {
  auto s = __builtin_amdgcn_permlane16_swap(__float_as_uint(x), __float_as_uint(x), false, false);
  return __uint_as_float(s[0]) + __uint_as_float(s[1]);
}
__device__ __forceinline__ float x32_sum(float x) {
  auto s = __builtin_amdgcn_permlane32_swap(__float_as_uint(x), __float_as_uint(x), false, false);
  return __uint_as_float(s[0]) + __uint_as_float(s[1]);
}
__device__ __forceinline__ float x16_max(float x) {
  auto s = __builtin_amdgcn_permlane16_swap(__float_as_uint(x), __float_as_uint(x), false, false);
  return fmaxf(__uint_as_float(s[0]), __uint_as_float(s[1]));
}
__device__ __forceinline__ float x32_max(float x) {
  auto s = __builtin_amdgcn_permlane32_swap(__float_as_uint(x), __float_as_uint(x), false, false);
  return fmaxf(__uint_as_float(s[0]), __uint_as_float(s[1]));
}
__device__ __forceinline__ float wave_sum(float x) {
  x += dpp<ROR8>(x); x += dpp<ROR4>(x); x += dpp<ROR2>(x); x += dpp<ROR1>(x);
  return x32_sum(x16_sum(x));
}
__device__ __forceinline__ unsigned pk(float lo, float hi) {
  f32x2 v = {lo, hi};
  return __builtin_bit_cast(unsigned, __builtin_convertvector(v, bf16x2));
}
__device__ __forceinline__ float lo16(unsigned v) { return __uint_as_float(v << 16); }
__device__ __forceinline__ float hi16(unsigned v) { return __uint_as_float(v & 0xffff0000u); }
__device__ __forceinline__ float rbf(float x) { return lo16(pk(x, x) ); }
__device__ __forceinline__ float silu(float x) { return x * __builtin_amdgcn_rcpf(1.f + __builtin_amdgcn_exp2f(-x * LOG2E)); }
__device__ __forceinline__ f32x4 mfma(v4u a, v4u b, f32x4 c) {
  return __builtin_amdgcn_mfma_f32_16x16x32_bf16(__builtin_bit_cast(bf16x8, a), __builtin_bit_cast(bf16x8, b), c, 0, 0, 0);
}
__device__ __forceinline__ v4u lds128(LAS unsigned char* L, unsigned off) { return *(LAS v4u*)(L + off); }
__device__ __forceinline__ v2u tr64(LAS unsigned char* L, unsigned off) {
  typedef short v4s __attribute__((ext_vector_type(4)));
  v4s r = __builtin_amdgcn_ds_read_tr16_b64_v4i16((LAS v4s*)(L + off));
  return __builtin_bit_cast(v2u, r);
}
__device__ __forceinline__ v4u trfrag(LAS unsigned char* L, unsigned off, unsigned dt) {
  const v2u a = tr64(L, off), b = tr64(L, off + dt);
  return v4u{a.x, a.y, b.x, b.y};
}
__device__ __forceinline__ float sel4(f32x4 d, unsigned u) {
  const float a = (u & 1) ? d[1] : d[0], b = (u & 1) ? d[3] : d[2];
  return (u & 2) ? b : a;
}
__device__ __forceinline__ void st_granule(unsigned char* ws, unsigned off, unsigned tag, unsigned val) {
  __hip_atomic_store((gu64*)(ws + off), ((u64)tag << 32) | val, __ATOMIC_RELAXED, __HIP_MEMORY_SCOPE_AGENT);
}
#define BAR() asm volatile("s_waitcnt lgkmcnt(0)\n\ts_barrier" ::: "memory")
#define VMWAIT(n) asm volatile("s_waitcnt vmcnt(" #n ")" ::: "memory")
#define DMA1(ldsa, v0, sb) \
  asm volatile("s_mov_b32 m0, %0\n\ts_nop 0\n\tglobal_load_lds_dwordx4 %1, %2" NTS ::"s"(ldsa), "v"(v0), "s"(sb) : "memory")
#define DMA2(ldsa, v0, v1, sb) \
  asm volatile("s_mov_b32 m0, %0\n\ts_nop 0\n\tglobal_load_lds_dwordx4 %1, %3" NTS "\n\tglobal_load_lds_dwordx4 %2, %3 offset:1024" NTS \
               ::"s"(ldsa), "v"(v0), "v"(v1), "s"(sb) : "memory")
#define DMA4(ldsa, v0, v1, v2, v3, sb) \
  asm volatile("s_mov_b32 m0, %0\n\ts_nop 0\n\tglobal_load_lds_dwordx4 %1, %5" NTS "\n\tglobal_load_lds_dwordx4 %2, %5 offset:1024" NTS \
               "\n\tglobal_load_lds_dwordx4 %3, %5 offset:2048" NTS "\n\tglobal_load_lds_dwordx4 %4, %5 offset:3072" NTS \
               ::"s"(ldsa), "v"(v0), "v"(v1), "v"(v2), "v"(v3), "s"(sb) : "memory")

#define ENC_ ADDX
#define STAMP(i) do { if ((i) == 0) t_ent = __builtin_amdgcn_s_memrealtime(); if ((i) == ENC_) t_enc = __builtin_amdgcn_s_memrealtime(); } while (0)
}

__global__ void __launch_bounds__(512, 2)
fused_decode_kernel(const u16* __restrict__ x, const u16* __restrict__ kc, const u16* __restrict__ vc, const u16* __restrict__ wq,
                    const u16* __restrict__ wo, const u16* __restrict__ w1, const u16* __restrict__ w3, const u16* __restrict__ w2,
                    u16* __restrict__ out, unsigned char* __restrict__ ws
) {
  __shared__ __attribute__((aligned(1024))) unsigned char smem[L_END];
  LAS unsigned char* L = (LAS unsigned char*)smem;
  const unsigned tid = threadIdx.x, lane = tid & 63;
  const unsigned w = __builtin_amdgcn_readfirstlane(tid >> 6);
  const unsigned c = blockIdx.x, g8 = c & 7, j = c >> 3;
  const unsigned b = j >> 3, sp = j & 7;
  u64 t_ent = 0, t_enc = 0;
  STAMP(0);
  asm volatile("" ::"s"(x), "s"(kc), "s"(vc), "s"(wq), "s"(wo), "s"(w1), "s"(w3), "s"(w2), "s"(out), "s"(ws));
  const unsigned l4 = lane >> 4, l15 = lane & 15;

  const bool isq = j < 16;
  const unsigned fidx = (j - 16) * 8 + g8;
  const bool hasw = isq || fidx < 112;
  const unsigned kh = isq ? (j >> 3) : (fidx & 1);
  const unsigned cm = fidx >> 1, mm = cm >= 28 ? 1 : 0, cbf = cm - 28 * mm;
  const unsigned wstride = isq ? 8192u : 3584u;
  const unsigned char* wbase = isq ? (const unsigned char*)wq + (size_t)kh * 256 * 8192 + g8 * 1024 + (j & 7) * 128
                                   : (const unsigned char*)(mm ? w3 : w1) + (size_t)kh * 256 * 3584 + cbf * 128;
  if (w < 4) DMA1(L_X + w * 1024, w * 1024 + lane * 16, x);
  if (hasw) {
    const unsigned r0 = 32 * w + (lane >> 3);
    const unsigned ch = (lane & 7) ^ (((lane >> 4) & 1) << 1);
    const unsigned v0 = r0 * wstride + ch * 16;
    const unsigned st = 8 * wstride - 1024;
    DMA4(L_W + w * 4096, v0, (v0 + st) ^ 64, v0 + 2 * st, (v0 + 3 * st) ^ 64, wbase);
  }
  const size_t kvoff = ((size_t)(b * 8 + g8) * SK + KOFF + sp * 128) * 256;
  asm volatile("s_barrier" ::: "memory");
  {
    const unsigned v0 = w * 4096 + l4 * 256 + ((l15 ^ l4) << 4);
    DMA4(L_K + w * 4096, v0, v0 ^ 64, v0 ^ 128, v0 ^ 192, (const unsigned char*)kc + kvoff);
  }
  const unsigned tag = *(const CAS unsigned*)(ws + WS_E) + 1u;
  STAMP(1);

  if (hasw && w < 4) {
    VMWAIT(8);
    const v4u xv = lds128(L, L_X + w * 1024 + lane * 16);
    float v[8];
#pragma unroll
    for (int i = 0; i < 4; i++) { v[2 * i] = lo16(xv[i]); v[2 * i + 1] = hi16(xv[i]); }
    float s = ((v[0] + v[1]) + (v[2] + v[3])) + ((v[4] + v[5]) + (v[6] + v[7]));
    const float mean = wave_sum(s) * (1.f / 512.f);
    float q = 0.f;
#pragma unroll
    for (int i = 0; i < 8; i++) { const float d = v[i] - mean; q = fmaf(d, d, q); }
    const float var = rbf(wave_sum(q) * (1.f / 512.f));
    const float den = rbf(__builtin_amdgcn_sqrtf(rbf(var + 1e-5f)));
    const float inv = 1.0f / den, mb = rbf(mean);
    v4u o;
#pragma unroll
    for (int i = 0; i < 4; i++) {
      const unsigned d2 = pk(v[2 * i] - mb, v[2 * i + 1] - mb);
      o[i] = pk(lo16(d2) * inv, hi16(d2) * inv);
    }
    *(LAS v4u*)(L + L_XN + w * XN_S + lane * 16) = o;
  }
  VMWAIT(4);
  BAR();
  STAMP(2);

  const unsigned q2 = l15 >> 2, p2 = lane & 3;
  const unsigned bb = lane & 3, uu = (lane >> 2) & 3;
  const unsigned vsw = w * 4096 + l4 * 256 + ((l15 ^ (l4 << 1)) << 4);
  const unsigned char* vbase = (const unsigned char*)vc + kvoff;
  if (w < 4) {
    if (hasw) {
      const unsigned f = (q2 >> 1) | ((l4 & 1) << 1);
      const unsigned ta = L_W + (8 * l4 + q2) * 128 + (((2 * w + (p2 >> 1)) ^ (f << 1)) << 4) + 8 * (p2 & 1);
      const unsigned xa = L_XN + bb * XN_S + kh * 512 + l4 * 16;
      f32x4 a0 = {0.f, 0.f, 0.f, 0.f}, a1 = {0.f, 0.f, 0.f, 0.f};
#pragma unroll
      for (int s = 0; s < 8; s += 2) {
        a0 = mfma(trfrag(L, ta + s * 4096, 512), lds128(L, xa + s * 64), a0);
        a1 = mfma(trfrag(L, ta + (s + 1) * 4096, 512), lds128(L, xa + (s + 1) * 64), a1);
      }
      const float r = sel4(a0 + a1, uu);
      const unsigned n = 16 * w + 4 * l4 + uu;
      unsigned off;
      if (isq) off = WS_QX + (((g8 * 4 + bb) * 2 + kh) * 512 + (j & 7) * 64 + n) * 8;
      else off = WS_HX + ((2 * cbf + (n >> 5)) * 512 + (mm * 2 + kh) * 128 + bb * 32 + (n & 31)) * 8;
      st_granule(ws, off, tag, __float_as_uint(r));
      STAMP(19);
    }
    DMA4(L_V + w * 4096, vsw, vsw, vsw ^ 128, vsw ^ 128, vbase);
  }
  STAMP(3);

  const unsigned rb = j >> 2, cc = j & 3;
  const bool has2 = rb < 7;
  const unsigned fb = g8 * 7 + (has2 ? rb : 0);
  const unsigned char* wob = (const unsigned char*)wo + (size_t)(g8 * 512 + rb * 64) * 1024 + cc * 256;
  const unsigned char* w2b = (const unsigned char*)w2 + (size_t)(fb * 32) * 1024 + cc * 256;
  const unsigned wov = (8 * w + l4) * 1024 + ((l15 ^ (l4 << 1) ^ ((w & 1) << 3)) << 4);
  const unsigned w2v = (4 * w + l4) * 1024 + ((l15 ^ (l4 << 1) ^ (((w >> 1) & 1) << 3)) << 4);
#define ISSUE_WO() do { DMA2(L_WO + w * 2048, wov, wov + 3072, wob); DMA1(L_W2 + w * 1024, w2v, w2b); } while (0)

  {
    const unsigned char* qb = ws + WS_QX + (g8 * 4 + b) * 8192 + 4096;
    const unsigned qo = tid * 8;
    u64 qa, qc;
    if (w < 4) {
      asm volatile("s_waitcnt vmcnt(4)\n\tglobal_load_dwordx2 %0, %2, %3 offset:-4096 sc1\n\tglobal_load_dwordx2 %1, %2, %3 sc1\n\ts_barrier\n\ts_waitcnt vmcnt(0)"
                   : "=&v"(qa), "=&v"(qc) : "v"(qo), "s"(qb) : "memory");
    } else {
      u64 ta, tc;
      asm volatile("s_waitcnt vmcnt(0)\n\tglobal_load_dwordx2 %0, %2, %3 offset:-4096 sc1\n\tglobal_load_dwordx2 %1, %2, %3 sc1\n\ts_barrier\n\t"
                   "s_mov_b32 m0, %4\n\ts_nop 0\n\tglobal_load_lds_dwordx4 %5, %7" NTS "\n\tglobal_load_lds_dwordx4 %5, %7 offset:1024" NTS
                   "\n\tglobal_load_lds_dwordx4 %6, %7 offset:2048" NTS "\n\tglobal_load_lds_dwordx4 %6, %7 offset:3072" NTS "\n\ts_waitcnt vmcnt(4)"
                   : "=&v"(ta), "=&v"(tc) : "v"(qo), "s"(qb), "s"(L_V + w * 4096), "v"(vsw), "v"(vsw ^ 128), "s"(vbase) : "memory");
      qa = ta; qc = tc;
    }
    STAMP(4);
    for (unsigned spin = 0; !__all((unsigned)(qa >> 32) == tag && (unsigned)(qc >> 32) == tag) && spin < SPIN_CAP; ++spin)
      asm volatile("global_load_dwordx2 %0, %2, %3 offset:-4096 sc1\n\tglobal_load_dwordx2 %1, %2, %3 sc1\n\ts_waitcnt vmcnt(0)"
                   : "=&v"(qa), "=&v"(qc) : "v"(qo), "s"(qb) : "memory");
    const float qv = __uint_as_float((unsigned)qa) + __uint_as_float((unsigned)qc);
    *(LAS u16*)(L + L_Q + (tid >> 7) * Q_S + (tid & 127) * 2) = (u16)pk(qv, qv);
  }
  BAR();
  STAMP(5);

  f32x4 sc = {0.f, 0.f, 0.f, 0.f};
  {
    const unsigned ka = L_K + (16 * w + l15) * 256 + ((l4 ^ l15) << 4);
    const unsigned qa = L_Q + bb * Q_S + l4 * 16;
#pragma unroll
    for (int s = 0; s < 4; s++) sc = mfma(lds128(L, ka ^ (s << 6)), lds128(L, qa + s * 64), sc);
  }
  asm volatile("" : "+v"(sc));
  STAMP(14);
  float m = fmaxf(fmaxf(sc[0], sc[1]), fmaxf(sc[2], sc[3]));
  m = x32_max(x16_max(m));
  if (lane < 4) *(LAS float*)(L + L_MX + lane * 32 + w * 4) = m;
  BAR();
  STAMP(15);
  {
    const f32x4 a = *(LAS f32x4*)(L + L_MX + bb * 32), d = *(LAS f32x4*)(L + L_MX + bb * 32 + 16);
    m = fmaxf(fmaxf(fmaxf(a[0], a[1]), fmaxf(a[2], a[3])), fmaxf(fmaxf(d[0], d[1]), fmaxf(d[2], d[3])));
  }
  const float m2 = m * C2;
  {
    float p[4];
#pragma unroll
    for (int i = 0; i < 4; i++) p[i] = __builtin_amdgcn_exp2f(fmaf(sc[i], C2, -m2));
    float ps = (p[0] + p[1]) + (p[2] + p[3]);
    ps = x32_sum(x16_sum(ps));
    if (lane < 4) *(LAS float*)(L + L_LS + lane * 32 + w * 4) = ps;
    if (uu == 0) *(LAS v2u*)(L + L_P + bb * P_S + (16 * w + 4 * l4) * 2) = v2u{pk(p[0], p[1]), pk(p[2], p[3])};
  }
  STAMP(16);
  ISSUE_WO();
  STAMP(17);
  BAR();
  STAMP(18);
  v4u pf[4];
#pragma unroll
  for (int s = 0; s < 4; s++) pf[s] = lds128(L, L_P + bb * P_S + s * 64 + l4 * 16);
  float lsum;
  {
    const f32x4 a = *(LAS f32x4*)(L + L_LS + bb * 32), d = *(LAS f32x4*)(L + L_LS + bb * 32 + 16);
    lsum = ((a[0] + a[1]) + (a[2] + a[3])) + ((d[0] + d[1]) + (d[2] + d[3]));
  }
  const float linv = __builtin_amdgcn_rcpf(lsum);
  const float lse = m2 + __builtin_amdgcn_logf(lsum);
  asm volatile("" : "+v"(pf[0]), "+v"(pf[1]), "+v"(pf[2]), "+v"(pf[3]));
  STAMP(6);

  VMWAIT(3);
  BAR();
  STAMP(7);
  const unsigned tb = (8 * l4 + q2) * 256 + (((2 * w + (p2 >> 1)) ^ (2 * q2) ^ ((l4 & 1) << 3)) << 4) + 8 * (p2 & 1);
  {
    f32x4 o0 = {0.f, 0.f, 0.f, 0.f}, o1 = {0.f, 0.f, 0.f, 0.f};
    o0 = mfma(trfrag(L, L_V + tb, 1024), pf[0], o0);
    o1 = mfma(trfrag(L, L_V + tb + 8192, 1024), pf[1], o1);
    o0 = mfma(trfrag(L, L_V + tb + 16384, 1024), pf[2], o0);
    o1 = mfma(trfrag(L, L_V + tb + 24576, 1024), pf[3], o1);
    const f32x4 o = (o0 + o1) * linv;
    const unsigned val = (uu & 1) ? pk(o[2], o[3]) : pk(o[0], o[1]);
    const unsigned rbo = 2 * bb + (w >> 2);
    if (uu < 2) st_granule(ws, WS_OX + ((((g8 * 8 + rbo) * 4 + b) * 8 + sp) * 32 + 8 * (w & 3) + 2 * l4 + uu) * 8, tag, val);
    if (w == 0 && lane < 8) st_granule(ws, WS_LX + (((g8 * 8 + 2 * bb + (lane >> 2)) * 4 + b) * 8 + sp) * 8, tag, __float_as_uint(lse));
  }
  STAMP(8);
  {
    const unsigned char* hb = ws + WS_HX + fb * 4096;
    const unsigned ho = ((tid & 3) * 128 + (tid >> 2)) * 8;
    u64 hv;
    unsigned spin = 0;
    do {
      asm volatile("global_load_dwordx2 %0, %1, %2 sc1\n\ts_waitcnt vmcnt(0)" : "=&v"(hv) : "v"(ho), "s"(hb) : "memory");
    } while (!__all((unsigned)(hv >> 32) == tag) && ++spin < SPIN_CAP);
    float y = __uint_as_float((unsigned)hv);
    y = rbf(y + dpp<XOR1>(y));
    const float up = dpp<XOR2>(y);
    const float hh = rbf(silu(y)) * up;
    if ((lane & 3) == 0) *(LAS u16*)(L + L_H + (tid >> 7) * H_S + ((tid >> 2) & 31) * 2) = (u16)pk(hh, hh);
  }
  STAMP(9);

  {
    const unsigned ob = w >> 1, half = w & 1, os = lane & 7, ii = lane >> 3;
    const unsigned char* obase = ws + WS_OX + (g8 * 8 + rb) * 8192;
    const unsigned char* lbase = ws + WS_LX + (g8 * 8 + rb) * 256;
    const unsigned oo = ((ob * 8 + os) * 32 + 16 * half + 2 * ii) * 8, lo = (ob * 8 + os) * 8;
    u64 o0, o1, ls;
    unsigned spin = 0;
    do {
      asm volatile("global_load_dwordx2 %0, %3, %4 sc1\n\tglobal_load_dwordx2 %1, %3, %4 offset:8 sc1\n\tglobal_load_dwordx2 %2, %5, %6 sc1\n\ts_waitcnt vmcnt(0)"
                   : "=&v"(o0), "=&v"(o1), "=&v"(ls) : "v"(oo), "s"(obase), "v"(lo), "s"(lbase) : "memory");
    } while (!__all((unsigned)(o0 >> 32) == tag && (unsigned)(o1 >> 32) == tag && (unsigned)(ls >> 32) == tag) && ++spin < SPIN_CAP);
    STAMP(10);
    const float lv = __uint_as_float((unsigned)ls);
    float mx = fmaxf(lv, dpp<XOR1>(lv)); mx = fmaxf(mx, dpp<XOR2>(mx)); mx = fmaxf(mx, dpp<HMIR>(mx));
    const float e = __builtin_amdgcn_exp2f(lv - mx);
    float es = e + dpp<XOR1>(e); es += dpp<XOR2>(es); es += dpp<HMIR>(es);
    const float wt = e * __builtin_amdgcn_rcpf(es);
    float v[4] = {lo16((unsigned)o0) * wt, hi16((unsigned)o0) * wt, lo16((unsigned)o1) * wt, hi16((unsigned)o1) * wt};
#pragma unroll
    for (int i = 0; i < 4; i++) { v[i] += dpp<XOR1>(v[i]); v[i] += dpp<XOR2>(v[i]); v[i] += dpp<HMIR>(v[i]); }
    if (os == 0) *(LAS v2u*)(L + L_O + ob * O_S + 64 * half + 8 * ii) = v2u{pk(v[0], v[1]), pk(v[2], v[3])};
  }
  STAMP(20);
  BAR();
  STAMP(21);
  {
    const v4u a0 = trfrag(L, L_WO + tb, 1024), a1 = trfrag(L, L_WO + tb + 8192, 1024), a2 = trfrag(L, L_W2 + tb, 1024);
    const unsigned oa = L_O + bb * O_S + l4 * 16;
    f32x4 s0 = {0.f, 0.f, 0.f, 0.f}, s1 = {0.f, 0.f, 0.f, 0.f}, g0 = {0.f, 0.f, 0.f, 0.f};
    s0 = mfma(a0, lds128(L, oa), s0);
    s1 = mfma(a1, lds128(L, oa + 64), s1);
    g0 = mfma(a2, lds128(L, L_H + bb * H_S + l4 * 16), g0);
    const unsigned n = cc * 128 + 16 * w + 4 * l4 + uu;
    const unsigned o = (bb * 64 + (n >> 3)) * 512 + (n & 7);
    st_granule(ws, WS_SX + (o + (g8 * 8 + rb) * 8) * 8, tag, __float_as_uint(sel4(s0 + s1, uu)));
    if (has2) st_granule(ws, WS_GX + (o + fb * 8) * 8, tag, __float_as_uint(sel4(g0, uu)));
  }
  STAMP(11);

  {
    const unsigned char* sb = ws + WS_SX + c * 4096;
    const unsigned char* gb = ws + WS_GX + c * 4096;
    const unsigned so = tid * 8, go = (tid < 448 ? tid : tid - 64) * 8;
    u64 sv, gv;
    unsigned spin = 0;
    VMWAIT(0);
    do {
      asm volatile("global_load_dwordx2 %0, %2, %3 sc1\n\tglobal_load_dwordx2 %1, %4, %5 sc1\n\ts_waitcnt vmcnt(0)"
                   : "=&v"(sv), "=&v"(gv) : "v"(so), "s"(sb), "v"(go), "s"(gb) : "memory");
    } while (!__all((unsigned)(sv >> 32) == tag && (unsigned)(gv >> 32) == tag) && ++spin < SPIN_CAP);
    STAMP(12);
    float s = __uint_as_float((unsigned)sv), gg = w < 7 ? __uint_as_float((unsigned)gv) : 0.f;
    s += dpp<ROR8>(s); gg += dpp<ROR8>(gg);
    s = x32_sum(x16_sum(s)); gg = x32_sum(x16_sum(gg));
    if (lane < 8) {
      *(LAS float*)(L + L_FR + lane * 32 + w * 4) = s;
      *(LAS float*)(L + L_FR + 256 + lane * 32 + w * 4) = gg;
    }
  }
  BAR();
  if (tid < 8) {
    const f32x4 a = *(LAS f32x4*)(L + L_FR + lane * 32), d = *(LAS f32x4*)(L + L_FR + lane * 32 + 16);
    const f32x4 e = *(LAS f32x4*)(L + L_FR + 256 + lane * 32), f = *(LAS f32x4*)(L + L_FR + 256 + lane * 32 + 16);
    const float s = rbf(((a[0] + a[1]) + (a[2] + a[3])) + ((d[0] + d[1]) + (d[2] + d[3])));
    const float gg = rbf(((e[0] + e[1]) + (e[2] + e[3])) + ((f[0] + f[1]) + (f[2] + f[3])));
    const float xv = lo16(*(LAS u16*)(L + L_X + c * 16 + lane * 2));
    const float r = rbf(s * rbf(silu(gg)));
    out[c * 8 + lane] = (u16)pk(xv + r, xv + r);
    if (c == 0 && tid == 0) __hip_atomic_store((gu32*)(ws + WS_E), tag, __ATOMIC_RELAXED, __HIP_MEMORY_SCOPE_AGENT);
  }
  STAMP(13);
  { const u64 tn = __builtin_amdgcn_s_memrealtime(); while (__builtin_amdgcn_s_memrealtime() < tn + (t_enc - t_ent)) __builtin_amdgcn_s_sleep(1); }
}

extern "C" void kernel_launch(void* const* d_in, const int* in_sizes, int n_in, void* d_out, int out_size, void* d_ws, size_t ws_size,
                              hipStream_t stream) {
  (void)in_sizes; (void)n_in; (void)out_size; (void)ws_size;
  fused_decode_kernel<<<256, 512, 0, stream>>>((const u16*)d_in[0], (const u16*)d_in[1], (const u16*)d_in[2], (const u16*)d_in[3],
                                               (const u16*)d_in[4], (const u16*)d_in[6], (const u16*)d_in[7], (const u16*)d_in[8],
                                               (u16*)d_out, (unsigned char*)d_ws);
}
```

```cpp
#define FIXT 3000
#define ADDX 99
#define ONLY0 1
#include <hip/hip_runtime.h>
#include <hip/hip_bf16.h>

#define GAS __attribute__((address_space(1)))
#define LAS __attribute__((address_space(3)))
#define CAS __attribute__((address_space(4)))
typedef unsigned short u16;
typedef unsigned long long u64;
typedef unsigned v4u __attribute__((ext_vector_type(4)));
typedef unsigned v2u __attribute__((ext_vector_type(2)));
typedef float f32x4 __attribute__((ext_vector_type(4)));
typedef float f32x2 __attribute__((ext_vector_type(2)));
typedef short s16x4 __attribute__((ext_vector_type(4)));
typedef __bf16 bf16x8 __attribute__((ext_vector_type(8)));
typedef __bf16 bf16x2 __attribute__((ext_vector_type(2)));
typedef GAS u64 gu64;
typedef GAS unsigned gu32;

#define WO_AT 1
#define NTS " nt"

namespace {
constexpr int SK = 4096, KOFF = 3072, FF = 1792;
constexpr unsigned WS_E = 0;
constexpr unsigned WS_QX = 4096;
constexpr unsigned WS_HX = WS_QX + 8 * 4 * 2 * 512 * 8;
constexpr unsigned WS_OX = WS_HX + 56 * 512 * 8;
constexpr unsigned WS_LX = WS_OX + 64 * 1024 * 8;
constexpr unsigned WS_SX = WS_LX + 64 * 32 * 8;
constexpr unsigned WS_GX = WS_SX + 256 * 512 * 8;
constexpr unsigned WS_END = WS_GX + 256 * 512 * 8;
constexpr unsigned L_W = 0, L_K = 32768, L_V = 65536, L_WO = 98304, L_W2 = 114688, L_X = 122880;
constexpr unsigned L_XN = 126976, XN_S = 1056;
constexpr unsigned L_Q = L_XN + 4 * XN_S, Q_S = 288;
constexpr unsigned L_P = L_Q + 4 * Q_S, P_S = 288;
constexpr unsigned L_O = L_P + 4 * P_S, O_S = 160;
constexpr unsigned L_H = L_O + 4 * O_S, H_S = 96;
constexpr unsigned L_MX = L_H + 4 * H_S;
constexpr unsigned L_LS = L_MX + 128;
constexpr unsigned L_FR = L_LS + 128;
constexpr unsigned L_PF = L_FR + 512;
constexpr unsigned L_END = L_PF + 1024;
constexpr unsigned SPIN_CAP = 1u << 17;
constexpr float C2 = 0.088388347648318440f * 1.4426950408889634f;
constexpr float LOG2E = 1.4426950408889634f;

template <int CTRL> __device__ __forceinline__ float dpp(float x) {
  return __builtin_bit_cast(float, __builtin_amdgcn_mov_dpp(__builtin_bit_cast(int, x), CTRL, 0xf, 0xf, true));
}
constexpr int XOR1 = 0xB1, XOR2 = 0x4E, HMIR = 0x141, ROR8 = 0x128, ROR4 = 0x124, ROR2 = 0x122, ROR1 = 0x121;
__device__ __forceinline__ float x16_sum(float x) {
  auto s = __builtin_amdgcn_permlane16_swap(__float_as_uint(x), __float_as_uint(x), false, false);
  return __uint_as_float(s[0]) + __uint_as_float(s[1]);
}
__device__ __forceinline__ float x32_sum(float x) {
  auto s = __builtin_amdgcn_permlane32_swap(__float_as_uint(x), __float_as_uint(x), false, false);
  return __uint_as_float(s[0]) + __uint_as_float(s[1]);
}
__device__ __forceinline__ float x16_max(float x) {
  auto s = __builtin_amdgcn_permlane16_swap(__float_as_uint(x), __float_as_uint(x), false, false);
  return fmaxf(__uint_as_float(s[0]), __uint_as_float(s[1]));
}
__device__ __forceinline__ float x32_max(float x) {
  auto s = __builtin_amdgcn_permlane32_swap(__float_as_uint(x), __float_as_uint(x), false, false);
  return fmaxf(__uint_as_float(s[0]), __uint_as_float(s[1]));
}
__device__ __forceinline__ float wave_sum(float x) {
  x += dpp<ROR8>(x); x += dpp<ROR4>(x); x += dpp<ROR2>(x); x += dpp<ROR1>(x);
  return x32_sum(x16_sum(x));
}
__device__ __forceinline__ unsigned pk(float lo, float hi) {
  f32x2 v = {lo, hi};
  return __builtin_bit_cast(unsigned, __builtin_convertvector(v, bf16x2));
}
__device__ __forceinline__ float lo16(unsigned v) { return __uint_as_float(v << 16); }
__device__ __forceinline__ float hi16(unsigned v) { return __uint_as_float(v & 0xffff0000u); }
__device__ __forceinline__ float rbf(float x) { return lo16(pk(x, x) ); }
__device__ __forceinline__ float silu(float x) { return x * __builtin_amdgcn_rcpf(1.f + __builtin_amdgcn_exp2f(-x * LOG2E)); }
__device__ __forceinline__ f32x4 mfma(v4u a, v4u b, f32x4 c) {
  return __builtin_amdgcn_mfma_f32_16x16x32_bf16(__builtin_bit_cast(bf16x8, a), __builtin_bit_cast(bf16x8, b), c, 0, 0, 0);
}
__device__ __forceinline__ v4u lds128(LAS unsigned char* L, unsigned off) { return *(LAS v4u*)(L + off); }
__device__ __forceinline__ v2u tr64(LAS unsigned char* L, unsigned off) {
  typedef short v4s __attribute__((ext_vector_type(4)));
  v4s r = __builtin_amdgcn_ds_read_tr16_b64_v4i16((LAS v4s*)(L + off));
  return __builtin_bit_cast(v2u, r);
}
__device__ __forceinline__ v4u trfrag(LAS unsigned char* L, unsigned off, unsigned dt) {
  const v2u a = tr64(L, off), b = tr64(L, off + dt);
  return v4u{a.x, a.y, b.x, b.y};
}
__device__ __forceinline__ float sel4(f32x4 d, unsigned u) {
  const float a = (u & 1) ? d[1] : d[0], b = (u & 1) ? d[3] : d[2];
  return (u & 2) ? b : a;
}
__device__ __forceinline__ void st_granule(unsigned char* ws, unsigned off, unsigned tag, unsigned val) {
  __hip_atomic_store((gu64*)(ws + off), ((u64)tag << 32) | val, __ATOMIC_RELAXED, __HIP_MEMORY_SCOPE_AGENT);
}
#define BAR() asm volatile("s_waitcnt lgkmcnt(0)\n\ts_barrier" ::: "memory")
#define VMWAIT(n) asm volatile("s_waitcnt vmcnt(" #n ")" ::: "memory")
#define DMA1(ldsa, v0, sb) \
  asm volatile("s_mov_b32 m0, %0\n\ts_nop 0\n\tglobal_load_lds_dwordx4 %1, %2" NTS ::"s"(ldsa), "v"(v0), "s"(sb) : "memory")
#define DMA2(ldsa, v0, v1, sb) \
  asm volatile("s_mov_b32 m0, %0\n\ts_nop 0\n\tglobal_load_lds_dwordx4 %1, %3" NTS "\n\tglobal_load_lds_dwordx4 %2, %3 offset:1024" NTS \
               ::"s"(ldsa), "v"(v0), "v"(v1), "s"(sb) : "memory")
#define DMA4(ldsa, v0, v1, v2, v3, sb) \
  asm volatile("s_mov_b32 m0, %0\n\ts_nop 0\n\tglobal_load_lds_dwordx4 %1, %5" NTS "\n\tglobal_load_lds_dwordx4 %2, %5 offset:1024" NTS \
               "\n\tglobal_load_lds_dwordx4 %3, %5 offset:2048" NTS "\n\tglobal_load_lds_dwordx4 %4, %5 offset:3072" NTS \
               ::"s"(ldsa), "v"(v0), "v"(v1), "v"(v2), "v"(v3), "s"(sb) : "memory")

#define ENC_ ADDX
#define STAMP(i) do { if ((i) == 0) t_ent = __builtin_amdgcn_s_memrealtime(); if ((i) == ENC_) t_enc = __builtin_amdgcn_s_memrealtime(); } while (0)
}

__global__ void __launch_bounds__(512, 2)
fused_decode_kernel(const u16* __restrict__ x, const u16* __restrict__ kc, const u16* __restrict__ vc, const u16* __restrict__ wq,
                    const u16* __restrict__ wo, const u16* __restrict__ w1, const u16* __restrict__ w3, const u16* __restrict__ w2,
                    u16* __restrict__ out, unsigned char* __restrict__ ws
) {
  __shared__ __attribute__((aligned(1024))) unsigned char smem[L_END];
  LAS unsigned char* L = (LAS unsigned char*)smem;
  const unsigned tid = threadIdx.x, lane = tid & 63;
  const unsigned w = __builtin_amdgcn_readfirstlane(tid >> 6);
  const unsigned c = blockIdx.x, g8 = c & 7, j = c >> 3;
  const unsigned b = j >> 3, sp = j & 7;
  u64 t_ent = 0, t_enc = 0;
  STAMP(0);
  asm volatile("" ::"s"(x), "s"(kc), "s"(vc), "s"(wq), "s"(wo), "s"(w1), "s"(w3), "s"(w2), "s"(out), "s"(ws));
  const unsigned l4 = lane >> 4, l15 = lane & 15;

  const bool isq = j < 16;
  const unsigned fidx = (j - 16) * 8 + g8;
  const bool hasw = isq || fidx < 112;
  const unsigned kh = isq ? (j >> 3) : (fidx & 1);
  const unsigned cm = fidx >> 1, mm = cm >= 28 ? 1 : 0, cbf = cm - 28 * mm;
  const unsigned wstride = isq ? 8192u : 3584u;
  const unsigned char* wbase = isq ? (const unsigned char*)wq + (size_t)kh * 256 * 8192 + g8 * 1024 + (j & 7) * 128
                                   : (const unsigned char*)(mm ? w3 : w1) + (size_t)kh * 256 * 3584 + cbf * 128;
  if (w < 4) DMA1(L_X + w * 1024, w * 1024 + lane * 16, x);
  if (hasw) {
    const unsigned r0 = 32 * w + (lane >> 3);
    const unsigned ch = (lane & 7) ^ (((lane >> 4) & 1) << 1);
    const unsigned v0 = r0 * wstride + ch * 16;
    const unsigned st = 8 * wstride - 1024;
    DMA4(L_W + w * 4096, v0, (v0 + st) ^ 64, v0 + 2 * st, (v0 + 3 * st) ^ 64, wbase);
  }
  const size_t kvoff = ((size_t)(b * 8 + g8) * SK + KOFF + sp * 128) * 256;
  asm volatile("s_barrier" ::: "memory");
  {
    const unsigned v0 = w * 4096 + l4 * 256 + ((l15 ^ l4) << 4);
    DMA4(L_K + w * 4096, v0, v0 ^ 64, v0 ^ 128, v0 ^ 192, (const unsigned char*)kc + kvoff);
  }
  const unsigned tag = *(const CAS unsigned*)(ws + WS_E) + 1u;
  STAMP(1);

  if (hasw && w < 4) {
    VMWAIT(8);
    const v4u xv = lds128(L, L_X + w * 1024 + lane * 16);
    float v[8];
#pragma unroll
    for (int i = 0; i < 4; i++) { v[2 * i] = lo16(xv[i]); v[2 * i + 1] = hi16(xv[i]); }
    float s = ((v[0] + v[1]) + (v[2] + v[3])) + ((v[4] + v[5]) + (v[6] + v[7]));
    const float mean = wave_sum(s) * (1.f / 512.f);
    float q = 0.f;
#pragma unroll
    for (int i = 0; i < 8; i++) { const float d = v[i] - mean; q = fmaf(d, d, q); }
    const float var = rbf(wave_sum(q) * (1.f / 512.f));
    const float den = rbf(__builtin_amdgcn_sqrtf(rbf(var + 1e-5f)));
    const float inv = 1.0f / den, mb = rbf(mean);
    v4u o;
#pragma unroll
    for (int i = 0; i < 4; i++) {
      const unsigned d2 = pk(v[2 * i] - mb, v[2 * i + 1] - mb);
      o[i] = pk(lo16(d2) * inv, hi16(d2) * inv);
    }
    *(LAS v4u*)(L + L_XN + w * XN_S + lane * 16) = o;
  }
  VMWAIT(4);
  BAR();
  STAMP(2);

  const unsigned q2 = l15 >> 2, p2 = lane & 3;
  const unsigned bb = lane & 3, uu = (lane >> 2) & 3;
  const unsigned vsw = w * 4096 + l4 * 256 + ((l15 ^ (l4 << 1)) << 4);
  const unsigned char* vbase = (const unsigned char*)vc + kvoff;
  if (w < 4) {
    if (hasw) {
      const unsigned f = (q2 >> 1) | ((l4 & 1) << 1);
      const unsigned ta = L_W + (8 * l4 + q2) * 128 + (((2 * w + (p2 >> 1)) ^ (f << 1)) << 4) + 8 * (p2 & 1);
      const unsigned xa = L_XN + bb * XN_S + kh * 512 + l4 * 16;
      f32x4 a0 = {0.f, 0.f, 0.f, 0.f}, a1 = {0.f, 0.f, 0.f, 0.f};
#pragma unroll
      for (int s = 0; s < 8; s += 2) {
        a0 = mfma(trfrag(L, ta + s * 4096, 512), lds128(L, xa + s * 64), a0);
        a1 = mfma(trfrag(L, ta + (s + 1) * 4096, 512), lds128(L, xa + (s + 1) * 64), a1);
      }
      const float r = sel4(a0 + a1, uu);
      const unsigned n = 16 * w + 4 * l4 + uu;
      unsigned off;
      if (isq) off = WS_QX + (((g8 * 4 + bb) * 2 + kh) * 512 + (j & 7) * 64 + n) * 8;
      else off = WS_HX + ((2 * cbf + (n >> 5)) * 512 + (mm * 2 + kh) * 128 + bb * 32 + (n & 31)) * 8;
      st_granule(ws, off, tag, __float_as_uint(r));
      STAMP(19);
    }
    DMA4(L_V + w * 4096, vsw, vsw, vsw ^ 128, vsw ^ 128, vbase);
  }
  STAMP(3);

  const unsigned rb = j >> 2, cc = j & 3;
  const bool has2 = rb < 7;
  const unsigned fb = g8 * 7 + (has2 ? rb : 0);
  const unsigned char* wob = (const unsigned char*)wo + (size_t)(g8 * 512 + rb * 64) * 1024 + cc * 256;
  const unsigned char* w2b = (const unsigned char*)w2 + (size_t)(fb * 32) * 1024 + cc * 256;
  const unsigned wov = (8 * w + l4) * 1024 + ((l15 ^ (l4 << 1) ^ ((w & 1) << 3)) << 4);
  const unsigned w2v = (4 * w + l4) * 1024 + ((l15 ^ (l4 << 1) ^ (((w >> 1) & 1) << 3)) << 4);
#define ISSUE_WO() do { DMA2(L_WO + w * 2048, wov, wov + 3072, wob); DMA1(L_W2 + w * 1024, w2v, w2b); } while (0)

  {
    const unsigned char* qb = ws + WS_QX + (g8 * 4 + b) * 8192 + 4096;
    const unsigned qo = tid * 8;
    u64 qa, qc;
    if (w < 4) {
      asm volatile("s_waitcnt vmcnt(4)\n\tglobal_load_dwordx2 %0, %2, %3 offset:-4096 sc1\n\tglobal_load_dwordx2 %1, %2, %3 sc1\n\ts_barrier\n\ts_waitcnt vmcnt(0)"
                   : "=&v"(qa), "=&v"(qc) : "v"(qo), "s"(qb) : "memory");
    } else {
      u64 ta, tc;
      asm volatile("s_waitcnt vmcnt(0)\n\tglobal_load_dwordx2 %0, %2, %3 offset:-4096 sc1\n\tglobal_load_dwordx2 %1, %2, %3 sc1\n\ts_barrier\n\t"
                   "s_mov_b32 m0, %4\n\ts_nop 0\n\tglobal_load_lds_dwordx4 %5, %7" NTS "\n\tglobal_load_lds_dwordx4 %5, %7 offset:1024" NTS
                   "\n\tglobal_load_lds_dwordx4 %6, %7 offset:2048" NTS "\n\tglobal_load_lds_dwordx4 %6, %7 offset:3072" NTS "\n\ts_waitcnt vmcnt(4)"
                   : "=&v"(ta), "=&v"(tc) : "v"(qo), "s"(qb), "s"(L_V + w * 4096), "v"(vsw), "v"(vsw ^ 128), "s"(vbase) : "memory");
      qa = ta; qc = tc;
    }
    STAMP(4);
    for (unsigned spin = 0; !__all((unsigned)(qa >> 32) == tag && (unsigned)(qc >> 32) == tag) && spin < SPIN_CAP; ++spin)
      asm volatile("global_load_dwordx2 %0, %2, %3 offset:-4096 sc1\n\tglobal_load_dwordx2 %1, %2, %3 sc1\n\ts_waitcnt vmcnt(0)"
                   : "=&v"(qa), "=&v"(qc) : "v"(qo), "s"(qb) : "memory");
    const float qv = __uint_as_float((unsigned)qa) + __uint_as_float((unsigned)qc);
    *(LAS u16*)(L + L_Q + (tid >> 7) * Q_S + (tid & 127) * 2) = (u16)pk(qv, qv);
  }
  BAR();
  STAMP(5);

  f32x4 sc = {0.f, 0.f, 0.f, 0.f};
  {
    const unsigned ka = L_K + (16 * w + l15) * 256 + ((l4 ^ l15) << 4);
    const unsigned qa = L_Q + bb * Q_S + l4 * 16;
#pragma unroll
    for (int s = 0; s < 4; s++) sc = mfma(lds128(L, ka ^ (s << 6)), lds128(L, qa + s * 64), sc);
  }
  asm volatile("" : "+v"(sc));
  STAMP(14);
  float m = fmaxf(fmaxf(sc[0], sc[1]), fmaxf(sc[2], sc[3]));
  m = x32_max(x16_max(m));
  if (lane < 4) *(LAS float*)(L + L_MX + lane * 32 + w * 4) = m;
  BAR();
  STAMP(15);
  {
    const f32x4 a = *(LAS f32x4*)(L + L_MX + bb * 32), d = *(LAS f32x4*)(L + L_MX + bb * 32 + 16);
    m = fmaxf(fmaxf(fmaxf(a[0], a[1]), fmaxf(a[2], a[3])), fmaxf(fmaxf(d[0], d[1]), fmaxf(d[2], d[3])));
  }
  const float m2 = m * C2;
  {
    float p[4];
#pragma unroll
    for (int i = 0; i < 4; i++) p[i] = __builtin_amdgcn_exp2f(fmaf(sc[i], C2, -m2));
    float ps = (p[0] + p[1]) + (p[2] + p[3]);
    ps = x32_sum(x16_sum(ps));
    if (lane < 4) *(LAS float*)(L + L_LS + lane * 32 + w * 4) = ps;
    if (uu == 0) *(LAS v2u*)(L + L_P + bb * P_S + (16 * w + 4 * l4) * 2) = v2u{pk(p[0], p[1]), pk(p[2], p[3])};
  }
  STAMP(16);
  ISSUE_WO();
  STAMP(17);
  BAR();
  STAMP(18);
  v4u pf[4];
#pragma unroll
  for (int s = 0; s < 4; s++) pf[s] = lds128(L, L_P + bb * P_S + s * 64 + l4 * 16);
  float lsum;
  {
    const f32x4 a = *(LAS f32x4*)(L + L_LS + bb * 32), d = *(LAS f32x4*)(L + L_LS + bb * 32 + 16);
    lsum = ((a[0] + a[1]) + (a[2] + a[3])) + ((d[0] + d[1]) + (d[2] + d[3]));
  }
  const float linv = __builtin_amdgcn_rcpf(lsum);
  const float lse = m2 + __builtin_amdgcn_logf(lsum);
  asm volatile("" : "+v"(pf[0]), "+v"(pf[1]), "+v"(pf[2]), "+v"(pf[3]));
  STAMP(6);

  VMWAIT(3);
  BAR();
  STAMP(7);
  const unsigned tb = (8 * l4 + q2) * 256 + (((2 * w + (p2 >> 1)) ^ (2 * q2) ^ ((l4 & 1) << 3)) << 4) + 8 * (p2 & 1);
  {
    f32x4 o0 = {0.f, 0.f, 0.f, 0.f}, o1 = {0.f, 0.f, 0.f, 0.f};
    o0 = mfma(trfrag(L, L_V + tb, 1024), pf[0], o0);
    o1 = mfma(trfrag(L, L_V + tb + 8192, 1024), pf[1], o1);
    o0 = mfma(trfrag(L, L_V + tb + 16384, 1024), pf[2], o0);
    o1 = mfma(trfrag(L, L_V + tb + 24576, 1024), pf[3], o1);
    const f32x4 o = (o0 + o1) * linv;
    const unsigned val = (uu & 1) ? pk(o[2], o[3]) : pk(o[0], o[1]);
    const unsigned rbo = 2 * bb + (w >> 2);
    if (uu < 2) st_granule(ws, WS_OX + ((((g8 * 8 + rbo) * 4 + b) * 8 + sp) * 32 + 8 * (w & 3) + 2 * l4 + uu) * 8, tag, val);
    if (w == 0 && lane < 8) st_granule(ws, WS_LX + (((g8 * 8 + 2 * bb + (lane >> 2)) * 4 + b) * 8 + sp) * 8, tag, __float_as_uint(lse));
  }
  STAMP(8);
  {
    const unsigned char* hb = ws + WS_HX + fb * 4096;
    const unsigned ho = ((tid & 3) * 128 + (tid >> 2)) * 8;
    u64 hv;
    unsigned spin = 0;
    do {
      asm volatile("global_load_dwordx2 %0, %1, %2 sc1\n\ts_waitcnt vmcnt(0)" : "=&v"(hv) : "v"(ho), "s"(hb) : "memory");
    } while (!__all((unsigned)(hv >> 32) == tag) && ++spin < SPIN_CAP);
    float y = __uint_as_float((unsigned)hv);
    y = rbf(y + dpp<XOR1>(y));
    const float up = dpp<XOR2>(y);
    const float hh = rbf(silu(y)) * up;
    if ((lane & 3) == 0) *(LAS u16*)(L + L_H + (tid >> 7) * H_S + ((tid >> 2) & 31) * 2) = (u16)pk(hh, hh);
  }
  STAMP(9);

  {
    const unsigned ob = w >> 1, half = w & 1, os = lane & 7, ii = lane >> 3;
    const unsigned char* obase = ws + WS_OX + (g8 * 8 + rb) * 8192;
    const unsigned char* lbase = ws + WS_LX + (g8 * 8 + rb) * 256;
    const unsigned oo = ((ob * 8 + os) * 32 + 16 * half + 2 * ii) * 8, lo = (ob * 8 + os) * 8;
    u64 o0, o1, ls;
    unsigned spin = 0;
    do {
      asm volatile("global_load_dwordx2 %0, %3, %4 sc1\n\tglobal_load_dwordx2 %1, %3, %4 offset:8 sc1\n\tglobal_load_dwordx2 %2, %5, %6 sc1\n\ts_waitcnt vmcnt(0)"
                   : "=&v"(o0), "=&v"(o1), "=&v"(ls) : "v"(oo), "s"(obase), "v"(lo), "s"(lbase) : "memory");
    } while (!__all((unsigned)(o0 >> 32) == tag && (unsigned)(o1 >> 32) == tag && (unsigned)(ls >> 32) == tag) && ++spin < SPIN_CAP);
    STAMP(10);
    const float lv = __uint_as_float((unsigned)ls);
    float mx = fmaxf(lv, dpp<XOR1>(lv)); mx = fmaxf(mx, dpp<XOR2>(mx)); mx = fmaxf(mx, dpp<HMIR>(mx));
    const float e = __builtin_amdgcn_exp2f(lv - mx);
    float es = e + dpp<XOR1>(e); es += dpp<XOR2>(es); es += dpp<HMIR>(es);
    const float wt = e * __builtin_amdgcn_rcpf(es);
    float v[4] = {lo16((unsigned)o0) * wt, hi16((unsigned)o0) * wt, lo16((unsigned)o1) * wt, hi16((unsigned)o1) * wt};
#pragma unroll
    for (int i = 0; i < 4; i++) { v[i] += dpp<XOR1>(v[i]); v[i] += dpp<XOR2>(v[i]); v[i] += dpp<HMIR>(v[i]); }
    if (os == 0) *(LAS v2u*)(L + L_O + ob * O_S + 64 * half + 8 * ii) = v2u{pk(v[0], v[1]), pk(v[2], v[3])};
  }
  STAMP(20);
  BAR();
  STAMP(21);
  {
    const v4u a0 = trfrag(L, L_WO + tb, 1024), a1 = trfrag(L, L_WO + tb + 8192, 1024), a2 = trfrag(L, L_W2 + tb, 1024);
    const unsigned oa = L_O + bb * O_S + l4 * 16;
    f32x4 s0 = {0.f, 0.f, 0.f, 0.f}, s1 = {0.f, 0.f, 0.f, 0.f}, g0 = {0.f, 0.f, 0.f, 0.f};
    s0 = mfma(a0, lds128(L, oa), s0);
    s1 = mfma(a1, lds128(L, oa + 64), s1);
    g0 = mfma(a2, lds128(L, L_H + bb * H_S + l4 * 16), g0);
    const unsigned n = cc * 128 + 16 * w + 4 * l4 + uu;
    const unsigned o = (bb * 64 + (n >> 3)) * 512 + (n & 7);
    st_granule(ws, WS_SX + (o + (g8 * 8 + rb) * 8) * 8, tag, __float_as_uint(sel4(s0 + s1, uu)));
    if (has2) st_granule(ws, WS_GX + (o + fb * 8) * 8, tag, __float_as_uint(sel4(g0, uu)));
  }
  STAMP(11);

  {
    const unsigned char* sb = ws + WS_SX + c * 4096;
    const unsigned char* gb = ws + WS_GX + c * 4096;
    const unsigned so = tid * 8, go = (tid < 448 ? tid : tid - 64) * 8;
    u64 sv, gv;
    unsigned spin = 0;
    VMWAIT(0);
    do {
      asm volatile("global_load_dwordx2 %0, %2, %3 sc1\n\tglobal_load_dwordx2 %1, %4, %5 sc1\n\ts_waitcnt vmcnt(0)"
                   : "=&v"(sv), "=&v"(gv) : "v"(so), "s"(sb), "v"(go), "s"(gb) : "memory");
    } while (!__all((unsigned)(sv >> 32) == tag && (unsigned)(gv >> 32) == tag) && ++spin < SPIN_CAP);
    STAMP(12);
    float s = __uint_as_float((unsigned)sv), gg = w < 7 ? __uint_as_float((unsigned)gv) : 0.f;
    s += dpp<ROR8>(s); gg += dpp<ROR8>(gg);
    s = x32_sum(x16_sum(s)); gg = x32_sum(x16_sum(gg));
    if (lane < 8) {
      *(LAS float*)(L + L_FR + lane * 32 + w * 4) = s;
      *(LAS float*)(L + L_FR + 256 + lane * 32 + w * 4) = gg;
    }
  }
  BAR();
  if (tid < 8) {
    const f32x4 a = *(LAS f32x4*)(L + L_FR + lane * 32), d = *(LAS f32x4*)(L + L_FR + lane * 32 + 16);
    const f32x4 e = *(LAS f32x4*)(L + L_FR + 256 + lane * 32), f = *(LAS f32x4*)(L + L_FR + 256 + lane * 32 + 16);
    const float s = rbf(((a[0] + a[1]) + (a[2] + a[3])) + ((d[0] + d[1]) + (d[2] + d[3])));
    const float gg = rbf(((e[0] + e[1]) + (e[2] + e[3])) + ((f[0] + f[1]) + (f[2] + f[3])));
    const float xv = lo16(*(LAS u16*)(L + L_X + c * 16 + lane * 2));
    const float r = rbf(s * rbf(silu(gg)));
    out[c * 8 + lane] = (u16)pk(xv + r, xv + r);
    if (c == 0 && tid == 0) __hip_atomic_store((gu32*)(ws + WS_E), tag, __ATOMIC_RELAXED, __HIP_MEMORY_SCOPE_AGENT);
  }
  STAMP(13);
  if (c == 0) while (__builtin_amdgcn_s_memrealtime() < t_ent + FIXT) __builtin_amdgcn_s_sleep(1);
}

extern "C" void kernel_launch(void* const* d_in, const int* in_sizes, int n_in, void* d_out, int out_size, void* d_ws, size_t ws_size,
                              hipStream_t stream) {
  (void)in_sizes; (void)n_in; (void)out_size; (void)ws_size;
  fused_decode_kernel<<<256, 512, 0, stream>>>((const u16*)d_in[0], (const u16*)d_in[1], (const u16*)d_in[2], (const u16*)d_in[3],
                                               (const u16*)d_in[4], (const u16*)d_in[6], (const u16*)d_in[7], (const u16*)d_in[8],
                                               (u16*)d_out, (unsigned char*)d_ws);
}
```

```cpp
#include <hip/hip_runtime.h>
#include <hip/hip_bf16.h>

#define GAS __attribute__((address_space(1)))
#define LAS __attribute__((address_space(3)))
#define CAS __attribute__((address_space(4)))
typedef unsigned short u16;
typedef unsigned long long u64;
typedef unsigned v4u __attribute__((ext_vector_type(4)));
typedef unsigned v2u __attribute__((ext_vector_type(2)));
typedef float f32x4 __attribute__((ext_vector_type(4)));
typedef float f32x2 __attribute__((ext_vector_type(2)));
typedef __bf16 bf16x8 __attribute__((ext_vector_type(8)));
typedef __bf16 bf16x2 __attribute__((ext_vector_type(2)));
typedef GAS u64 gu64;
typedef GAS unsigned gu32;

#define WINW 4
#define WINK0 4
#define WINK1 6
#define WINV 6
#define QSLEEP 32
#define QSLEEPN 96
#define OSLEEP 22
#define NTS " nt"

namespace {
constexpr int SK = 4096, KOFF = 3072;
constexpr unsigned WS_E = 0;
constexpr unsigned WS_QX = 4096;
constexpr unsigned WS_HX = WS_QX + 8 * 4 * 2 * 512 * 8;
constexpr unsigned WS_OX = WS_HX + 56 * 512 * 8;
constexpr unsigned WS_SG = WS_OX + 256 * 2048;
constexpr unsigned WS_END = WS_SG + 256 * 8192;
constexpr unsigned L_W = 0, L_K = 32768, L_V = 65536, L_WO = 98304, L_W2 = 114688, L_X = 122880;
static_assert(L_W2 + 1024 + 8192 - L_V < 65536, "one base register serves the V, Wo and w2 images");
constexpr unsigned L_XN = 126976, XN_S = 1056;
constexpr unsigned L_Q = L_XN + 4 * XN_S, Q_S = 288;
constexpr unsigned L_P = L_Q + 4 * Q_S, P_S = 288;
constexpr unsigned L_O = L_P + 4 * P_S, O_S = 160;
constexpr unsigned L_H = L_O + 4 * O_S, H_S = 96;
constexpr unsigned L_MX = L_H + 4 * H_S;
constexpr unsigned L_FR = L_MX + 128;
constexpr unsigned L_PF = L_FR + 512;
constexpr unsigned L_END = L_PF + 1024;
constexpr unsigned SPIN_CAP = 1u << 17;
constexpr float LOG2E = 1.4426950408889634f;
constexpr float C2 = 0.088388347648318440f * LOG2E;

template <int CTRL> __device__ __forceinline__ float dpp(float x) {
  return __builtin_bit_cast(float, __builtin_amdgcn_mov_dpp(__builtin_bit_cast(int, x), CTRL, 0xf, 0xf, true));
}
constexpr int XOR1 = 0xB1, XOR2 = 0x4E, ROR8 = 0x128, ROR4 = 0x124, ROR2 = 0x122, ROR1 = 0x121;
__device__ __forceinline__ float x16_sum(float x) {
  auto s = __builtin_amdgcn_permlane16_swap(__float_as_uint(x), __float_as_uint(x), false, false);
  return __uint_as_float(s[0]) + __uint_as_float(s[1]);
}
__device__ __forceinline__ float x32_sum(float x) {
  auto s = __builtin_amdgcn_permlane32_swap(__float_as_uint(x), __float_as_uint(x), false, false);
  return __uint_as_float(s[0]) + __uint_as_float(s[1]);
}
__device__ __forceinline__ float wave_sum(float x) {
  x += dpp<ROR8>(x); x += dpp<ROR4>(x); x += dpp<ROR2>(x); x += dpp<ROR1>(x);
  return x32_sum(x16_sum(x));
}
__device__ __forceinline__ float max3(float a, float b, float c) {
  float r;
  asm("v_max3_f32 %0, %1, %2, %3" : "=v"(r) : "v"(a), "v"(b), "v"(c));
  return r;
}
__device__ __forceinline__ float xrow_max(float x) {
  float t;
  asm("v_mov_b32 %1, %0\n\ts_nop 1\n\tv_permlane16_swap_b32 %0, %1\n\tv_max_f32 %0, %0, %1\n\t"
      "v_mov_b32 %1, %0\n\ts_nop 1\n\tv_permlane32_swap_b32 %0, %1\n\tv_max_f32 %0, %0, %1"
      : "+v"(x), "=&v"(t));
  return x;
}
__device__ __forceinline__ unsigned pk(float lo, float hi) {
  f32x2 v = {lo, hi};
  return __builtin_bit_cast(unsigned, __builtin_convertvector(v, bf16x2));
}
__device__ __forceinline__ float lo16(unsigned v) { return __uint_as_float(v << 16); }
__device__ __forceinline__ float hi16(unsigned v) { return __uint_as_float(v & 0xffff0000u); }
__device__ __forceinline__ float rbf(float x) { return lo16(pk(x, x)); }
__device__ __forceinline__ float silu(float x) { return x * __builtin_amdgcn_rcpf(1.f + __builtin_amdgcn_exp2f(-x * LOG2E)); }
__device__ __forceinline__ f32x4 mfma(v4u a, v4u b, f32x4 c) {
  return __builtin_amdgcn_mfma_f32_16x16x32_bf16(__builtin_bit_cast(bf16x8, a), __builtin_bit_cast(bf16x8, b), c, 0, 0, 0);
}
__device__ __forceinline__ v4u lds128(LAS unsigned char* L, unsigned off) { return *(LAS v4u*)(L + off); }
__device__ __forceinline__ v2u tr64(LAS unsigned char* L, unsigned off) {
  typedef short v4s __attribute__((ext_vector_type(4)));
  v4s r = __builtin_amdgcn_ds_read_tr16_b64_v4i16((LAS v4s*)(L + off));
  return __builtin_bit_cast(v2u, r);
}
__device__ __forceinline__ v4u trfrag(LAS unsigned char* L, unsigned off, unsigned dt) {
  const v2u a = tr64(L, off), b = tr64(L, off + dt);
  return v4u{a.x, a.y, b.x, b.y};
}
__device__ __forceinline__ float sel4(f32x4 d, unsigned u) {
  const float a = (u & 1) ? d[1] : d[0], b = (u & 1) ? d[3] : d[2];
  return (u & 2) ? b : a;
}
__device__ __forceinline__ void st_granule(unsigned char* ws, unsigned off, unsigned tag, unsigned val) {
  __hip_atomic_store((gu64*)(ws + off), ((u64)tag << 32) | val, __ATOMIC_RELAXED, __HIP_MEMORY_SCOPE_AGENT);
}
__device__ __forceinline__ void st_pair(unsigned char* ws, unsigned off, v4u v) {
  asm volatile("global_store_dwordx4 %0, %1, %2 sc1\n\ts_nop 1" ::"v"(off), "v"(v), "s"(ws) : "memory");
}
__device__ __forceinline__ bool any_bad(unsigned x) {
  asm volatile("" : "+v"(x));
  return __builtin_expect(__builtin_amdgcn_ballot_w64(x != 0u) != 0ull, 0);
}
#define BAR() asm volatile("s_waitcnt lgkmcnt(0)\n\ts_barrier" ::: "memory")
#define VMWAIT(n) asm volatile("s_waitcnt vmcnt(%0)" ::"i"(n) : "memory")
#define SB() __builtin_amdgcn_sched_barrier(0)
#define SLEEP(n) do { if ((n) > 0) __builtin_amdgcn_s_sleep(n); } while (0)
#define GATHER1(dst, off, base) \
  asm volatile("global_load_dwordx4 %0, %1, %2 sc1\n\ts_waitcnt vmcnt(0)" : "=&v"(dst) : "v"(off), "s"(base) : "memory")
#define DMA1(P, ldsa, v0, sb) \
  asm volatile("s_mov_b32 m0, %0\n\ts_nop 0\n\tglobal_load_lds_dwordx4 %1, %2" P ::"s"(ldsa), "v"(v0), "s"(sb) : "memory")
#define DMA2(P, ldsa, v0, v1, sb) \
  asm volatile("s_mov_b32 m0, %0\n\ts_nop 0\n\tglobal_load_lds_dwordx4 %1, %3" P "\n\tglobal_load_lds_dwordx4 %2, %3 offset:1024" P \
               ::"s"(ldsa), "v"(v0), "v"(v1), "s"(sb) : "memory")
#define DMA4W(ldsa, v0, v1, v2, v3, sb, win) \
  asm volatile("s_mov_b32 m0, %0\n\ts_waitcnt vmcnt(%6)\n\tglobal_load_lds_dwordx4 %1, %5" NTS "\n\ts_waitcnt vmcnt(%6)\n\tglobal_load_lds_dwordx4 %2, %5 offset:1024" NTS \
               "\n\ts_waitcnt vmcnt(%6)\n\tglobal_load_lds_dwordx4 %3, %5 offset:2048" NTS "\n\ts_waitcnt vmcnt(%6)\n\tglobal_load_lds_dwordx4 %4, %5 offset:3072" NTS \
               ::"s"(ldsa), "v"(v0), "v"(v1), "v"(v2), "v"(v3), "s"(sb), "i"((win) - 1) : "memory")

}

typedef unsigned v16u __attribute__((ext_vector_type(16)));
__device__ __forceinline__ const unsigned char* mkptr(unsigned lo, unsigned hi) { return (const unsigned char*)(((u64)hi << 32) | lo); }

__global__ void __launch_bounds__(512, 2)
fused_decode_kernel(const u16* x_, const u16* kc_, const u16* vc_, const u16* wq_, const u16* wo_, const u16* w1_, const u16* w3_,
                    const u16* w2_, u16* out_, unsigned char* ws_
) {
  v16u arg0; v4u arg1;
  unsigned c = blockIdx.x, tid = threadIdx.x;
  asm volatile("s_load_dwordx16 %0, %4, 0x0\n\ts_load_dwordx4 %1, %4, 0x40"
               : "=&s"(arg0), "=&s"(arg1), "+s"(c), "+v"(tid) : "s"(__builtin_amdgcn_kernarg_segment_ptr()));
  __shared__ __attribute__((aligned(1024))) unsigned char smem[L_END];
  LAS unsigned char* L = (LAS unsigned char*)smem;
  const unsigned lane = tid & 63;
  const unsigned w = __builtin_amdgcn_readfirstlane(tid >> 6), li = w & 3;
  const unsigned g8 = c & 7, j = c >> 3;
  const unsigned b = j >> 3, sp = j & 7;
  const unsigned l4 = lane >> 4, l15 = lane & 15;

  const bool isq = j < 16;
  const unsigned fidx = (c - 128) & 127;
  const bool hasw = isq || fidx < 112;
  const unsigned kh = isq ? (j >> 3) : (fidx & 1);
  const unsigned cm = fidx >> 1, mm = cm >= 28 ? 1 : 0, cbf = cm - 28 * mm;
  const unsigned wstride = isq ? 8192u : 3584u;
  unsigned woff = kh * 256 * wstride + (isq ? g8 * 1024 + (j & 7) * 128 : cbf * 128);
  unsigned wsel = isq ? 0 : 1 + mm;
  const unsigned wst = 8 * wstride - 1024;
  unsigned wst4 = 32 * wstride;
  unsigned wv0 = (64 * li + (lane >> 3)) * wstride + (((lane & 7) ^ (((lane >> 4) & 1) << 1)) << 4);
  unsigned wv1 = (wv0 + wst) ^ 64, wv2 = wv0 + 2 * wst, wv3 = (wv0 + 3 * wst) ^ 64;
  unsigned xo = w * 1024 + lane * 16, lw = L_W + li * 8192;
  asm volatile("" : "+v"(wv0), "+v"(wv1), "+v"(wv2), "+v"(wv3), "+v"(xo), "+s"(woff), "+s"(wsel), "+s"(wst4), "+s"(lw));
  asm volatile("s_waitcnt lgkmcnt(0)" : "+s"(arg0), "+s"(arg1));
  const unsigned char *x = mkptr(arg0[0], arg0[1]), *kc = mkptr(arg0[2], arg0[3]), *vc = mkptr(arg0[4], arg0[5]), *wq = mkptr(arg0[6], arg0[7]);
  const unsigned char *wo = mkptr(arg0[8], arg0[9]), *w1 = mkptr(arg0[10], arg0[11]), *w3 = mkptr(arg0[12], arg0[13]), *w2 = mkptr(arg0[14], arg0[15]);
  unsigned char* out = (unsigned char*)mkptr(arg1[0], arg1[1]);
  unsigned char* ws = (unsigned char*)mkptr(arg1[2], arg1[3]);
  const unsigned char* wbase = (wsel == 0 ? wq : wsel == 1 ? w1 : w3) + woff;
  unsigned lw4 = lw + 4096;
  asm volatile("" : "+s"(wbase), "+s"(lw4));
  if (w >= 4) {
    if (hasw) {
      DMA4W(lw, wv0, wv1, wv2, wv3, wbase, WINW);
      DMA4W(lw4, wv0 + wst4, wv1 + wst4, wv2 + wst4, wv3 + wst4, wbase, WINW);
    }
  } else {
    DMA1("", L_X + w * 1024, xo, x);
    if (w == 3) {
      u64 pc; unsigned len;
      asm volatile("s_getpc_b64 %0\n.Lfd_here%=:\n\ts_mov_b32 %1, .Lfd_end-.Lfd_here%=" : "=s"(pc), "=s"(len));
      const unsigned pfo = j * 1024 + lane * 16;
      if (pfo + 16 <= len)
        asm volatile("s_mov_b32 m0, %0\n\ts_nop 0\n\tglobal_load_lds_dwordx4 %1, %2" ::"s"(L_PF), "v"(pfo), "s"(pc & ~15ull) : "memory");
    }
  }
  const unsigned tag = *(const CAS unsigned*)(ws + WS_E) + 1u;

  const unsigned q2 = l15 >> 2, p2 = lane & 3;
  const unsigned bb = lane & 3, uu = (lane >> 2) & 3;
  const size_t kvoff = ((size_t)(b * 8 + g8) * SK + KOFF + sp * 128) * 256;
  const unsigned char* kbase = (const unsigned char*)kc + kvoff;
  const unsigned char* vbase = (const unsigned char*)vc + kvoff;
  unsigned k0 = li * 8192 + l4 * 256 + ((l15 ^ l4) << 4);
  unsigned u0 = li * 8192 + l4 * 256 + ((l15 ^ (l4 << 1)) << 4);
  unsigned ka = L_K + (16 * w + l15) * 256 + ((l4 ^ l15) << 4);
  unsigned fq = L_Q + bb * Q_S + l4 * 16;
  unsigned mxw = L_MX + (lane & 3) * 32 + w * 4, mxr = L_MX + bb * 32, pw2 = L_P + bb * P_S + (16 * w + 4 * l4) * 2;
  unsigned tbv = L_V + (8 * l4 + q2) * 256 + (((2 * w + (p2 >> 1)) ^ (2 * q2) ^ ((l4 & 1) << 3)) << 4) + 8 * (p2 & 1);
  unsigned po = WS_OX + (((g8 * 4 + b) * 8 + sp) * 256 + w * 32 + bb * 8 + 2 * l4 + uu) * 8;
  asm volatile("" : "+v"(k0), "+v"(u0), "+v"(ka), "+v"(fq), "+v"(mxw), "+v"(mxr), "+v"(pw2), "+v"(tbv), "+v"(po), "+s"(kbase), "+s"(vbase));
  const unsigned rb = j >> 2, cc = j & 3;
  const bool has2 = rb < 7;
  const unsigned fb = g8 * 7 + (has2 ? rb : 0);
  const unsigned t8 = tid & 255;
  const unsigned ep = tag << 16;

  if (w >= 4) {
    DMA4W(L_K + li * 8192, k0, k0 ^ 64, k0 ^ 128, k0 ^ 192, kbase, WINK0);
    VMWAIT(4);
    asm volatile("s_barrier" ::: "memory");
    DMA4W(L_K + li * 8192 + 4096, k0 + 4096, (k0 ^ 64) + 4096, (k0 ^ 128) + 4096, (k0 ^ 192) + 4096, kbase, WINK1);
    DMA4W(L_V + li * 8192, u0, u0, u0 ^ 128, u0 ^ 128, vbase, WINV);
    DMA4W(L_V + li * 8192 + 4096, u0 + 4096, u0 + 4096, (u0 ^ 128) + 4096, (u0 ^ 128) + 4096, vbase, WINV);
    VMWAIT(8);
    if (uu < 2) { st_granule(ws, po, ep - 65536u, 0u); st_granule(ws, po - 1024, ep - 65536u, 0u); }
  } else {
    const unsigned wf = (q2 >> 1) | ((l4 & 1) << 1);
    unsigned ta = L_W + (8 * l4 + q2) * 128 + (((2 * li + (p2 >> 1)) ^ (wf << 1)) << 4) + 8 * (p2 & 1);
    unsigned xa = L_XN + bb * XN_S + kh * 512 + l4 * 16;
    const unsigned pn = 16 * li + 4 * l4 + uu;
    unsigned pw = isq ? WS_QX + (((g8 * 4 + bb) * 2 + kh) * 512 + (j & 7) * 64 + pn) * 8
                      : WS_HX + ((2 * cbf + (pn >> 5)) * 512 + (mm * 2 + kh) * 128 + bb * 32 + (pn & 31)) * 8;
    const unsigned char* qb = ws + WS_QX + (g8 * 4 + b) * 8192 + 4096;
    unsigned qo = t8 * 16, ql = L_Q + (t8 >> 6) * Q_S + (t8 & 63) * 4;
    asm volatile("" : "+v"(ta), "+v"(xa), "+v"(pw), "+v"(qo), "+v"(ql), "+s"(qb));
    VMWAIT(0);
    if (hasw) {
      const v4u xv = lds128(L, L_X + xo);
      float v[8];
#pragma unroll
      for (int i = 0; i < 4; i++) { v[2 * i] = lo16(xv[i]); v[2 * i + 1] = hi16(xv[i]); }
      float s = ((v[0] + v[1]) + (v[2] + v[3])) + ((v[4] + v[5]) + (v[6] + v[7]));
      const float mean = wave_sum(s) * (1.f / 512.f);
      float q = 0.f;
#pragma unroll
      for (int i = 0; i < 8; i++) { const float d = v[i] - mean; q = fmaf(d, d, q); }
      const float var = rbf(wave_sum(q) * (1.f / 512.f));
      const float den = rbf(__builtin_amdgcn_sqrtf(rbf(var + 1e-5f)));
      const float inv = __builtin_amdgcn_rcpf(den), mb = rbf(mean);
      v4u o;
#pragma unroll
      for (int i = 0; i < 4; i++) {
        const unsigned d2 = pk(v[2 * i] - mb, v[2 * i + 1] - mb);
        o[i] = pk(lo16(d2) * inv, hi16(d2) * inv);
      }
      *(LAS v4u*)(L + L_XN + w * XN_S + lane * 16) = o;
    }
    BAR();
    if (hasw) {
      v4u af[8], bf[8];
#pragma unroll
      for (int s2 = 0; s2 < 8; s2++) { af[s2] = trfrag(L, ta + s2 * 4096, 512); bf[s2] = lds128(L, xa + s2 * 64); }
      SB();
      f32x4 a0 = {0.f, 0.f, 0.f, 0.f}, a1 = {0.f, 0.f, 0.f, 0.f};
#pragma unroll
      for (int s2 = 0; s2 < 8; s2 += 2) {
        a0 = mfma(af[s2], bf[s2], a0);
        a1 = mfma(af[s2 + 1], bf[s2 + 1], a1);
      }
      st_granule(ws, pw, tag, __float_as_uint(sel4(a0 + a1, uu)));
      SLEEP(QSLEEP);
    } else {
      SLEEP(QSLEEPN);
    }
    v4u qa, qc;
    for (unsigned spin = 0;; ++spin) {
      asm volatile("global_load_dwordx4 %0, %2, %3 offset:-4096 sc1\n\tglobal_load_dwordx4 %1, %2, %3 sc1\n\ts_waitcnt vmcnt(0)"
                   : "=&v"(qa), "=&v"(qc) : "v"(qo), "s"(qb) : "memory");
      if (!any_bad(((qa.y ^ tag) | (qa.w ^ tag)) | ((qc.y ^ tag) | (qc.w ^ tag))) || spin >= SPIN_CAP) break;
    }
    *(LAS unsigned*)(L + ql) = pk(__uint_as_float(qa.x) + __uint_as_float(qc.x), __uint_as_float(qa.z) + __uint_as_float(qc.z));
  }
  BAR();

  v4u kf[4], qf[4];
#pragma unroll
  for (int s = 0; s < 4; s++) { kf[s] = lds128(L, ka ^ (s << 6)); qf[s] = lds128(L, fq + s * 64); }
  SB();
  const unsigned char* hb = ws + WS_HX + fb * 4096;
  const unsigned go = ((((t8 >> 1) & 1) * 2 + (t8 & 1)) * 128 + (t8 >> 2) * 2) * 8;
  v4u hv = {0u, 0u, 0u, 0u};
  if (w < 4) asm volatile("global_load_dwordx4 %0, %1, %2 sc1" : "=&v"(hv) : "v"(go), "s"(hb) : "memory");
  SB();
  f32x4 sc = {0.f, 0.f, 0.f, 0.f}, sd = {0.f, 0.f, 0.f, 0.f};
  sc = mfma(kf[0], qf[0], sc);
  sd = mfma(kf[1], qf[1], sd);
  sc = mfma(kf[2], qf[2], sc);
  sd = mfma(kf[3], qf[3], sd);
  sc += sd;
  float m = xrow_max(max3(fmaxf(sc[0], sc[1]), sc[2], sc[3]));
  if (lane < 4) *(LAS float*)(L + mxw) = m;
  BAR();
  {
    const f32x4 a = *(LAS f32x4*)(L + mxr), d = *(LAS f32x4*)(L + mxr + 16);
    m = max3(max3(a[0], a[1], a[2]), max3(a[3], d[0], d[1]), max3(d[2], d[3], m));
  }
  float m2 = m * C2;
  {
    float p[4];
#pragma unroll
    for (int i = 0; i < 4; i++) p[i] = __builtin_amdgcn_exp2f(fmaf(sc[i], C2, -m2));
    if (uu == 0) *(LAS v2u*)(L + pw2) = v2u{pk(p[0], p[1]), pk(p[2], p[3])};
  }
  const unsigned char* wob = (const unsigned char*)wo + (size_t)(g8 * 512 + rb * 64) * 1024 + cc * 256;
  const unsigned char* w2b = (const unsigned char*)w2 + (size_t)(fb * 32) * 1024 + cc * 256;
  const unsigned wov = (8 * w + l4) * 1024 + ((l15 ^ (l4 << 1) ^ ((w & 1) << 3)) << 4);
  const unsigned w2v = (4 * w + l4) * 1024 + ((l15 ^ (l4 << 1) ^ (((w >> 1) & 1) << 3)) << 4);
  DMA2(NTS, L_WO + w * 2048, wov, wov + 3072, wob);
  DMA1(NTS, L_W2 + w * 1024, w2v, w2b);
  asm volatile("s_waitcnt vmcnt(3)" : "+v"(hv)::"memory");
  BAR();
  {
    const v4u p0 = lds128(L, fq + 4 * Q_S), p1 = lds128(L, fq + 4 * Q_S + 64), p2_ = lds128(L, fq + 4 * Q_S + 128), p3 = lds128(L, fq + 4 * Q_S + 192);
    const v4u a0 = trfrag(L, tbv, 1024), a1 = trfrag(L, tbv + 8192, 1024), a2 = trfrag(L, tbv + 16384, 1024), a3 = trfrag(L, tbv + 24576, 1024);
    SB();
    const v4u ones = {0x3f803f80u, 0x3f803f80u, 0x3f803f80u, 0x3f803f80u};
    f32x4 o0 = {0.f, 0.f, 0.f, 0.f}, o1 = {0.f, 0.f, 0.f, 0.f}, l0 = {0.f, 0.f, 0.f, 0.f}, l1 = {0.f, 0.f, 0.f, 0.f};
    l0 = mfma(ones, p0, l0);
    l1 = mfma(ones, p1, l1);
    l0 = mfma(ones, p2_, l0);
    l1 = mfma(ones, p3, l1);
    o0 = mfma(a0, p0, o0);
    o1 = mfma(a1, p1, o1);
    o0 = mfma(a2, p2_, o0);
    o1 = mfma(a3, p3, o1);
    const float lsum = l0[0] + l1[0];
    const float linv = __builtin_amdgcn_rcpf(lsum);
    const unsigned lse = __float_as_uint(m2 + __builtin_amdgcn_logf(lsum));
    const unsigned tf = ep | ((uu & 1) ? (lse & 0xffffu) : (lse >> 16));
    const f32x4 o = o0 + o1;
    const float e0 = ((uu & 1) ? o[2] : o[0]) * linv, e1 = ((uu & 1) ? o[3] : o[1]) * linv;
    if (uu < 2) st_granule(ws, po, tf, pk(e0, e1));
  }
  unsigned ln = lane, wl = w, cl = c;
  asm volatile("" : "+v"(ln), "+s"(wl), "+s"(cl));
  const unsigned gl8 = cl & 7, jl = cl >> 3, rbl = jl >> 2, l4l = ln >> 4, bl = ln & 3, ul = (ln >> 2) & 3;
  const unsigned fo = gl8 * 32 + jl;
  const unsigned ob = wl >> 1, half = wl & 1, os = ln & 7, ii = ln >> 3;
  const unsigned char* obase = ws + WS_OX + gl8 * 65536 + (rbl & 1) * 1024 + (rbl >> 1) * 64;
  unsigned oo = (ob * 8 + os) * 2048 + (2 * half + (ii >> 2)) * 256 + (ii & 3) * 16;
  unsigned ow = L_O + ob * O_S + 64 * half + 8 * ii;
  unsigned fo_ = L_O + bl * O_S + l4l * 16, fh = L_H + bl * H_S + l4l * 16;
  unsigned psg = WS_SG + ((bl * 64 + (jl & 3) * 16 + 2 * wl + (l4l >> 1)) * 64 + gl8 * 8 + rbl) * 128 + (4 * (l4l & 1) + ul) * 16;
  const unsigned char* sgb = ws + WS_SG + fo * 8192;
  unsigned so = (wl * 64 + ln) * 16, frw = L_FR + (ln & 7) * 32 + wl * 4, frr = L_FR + (ln & 15) * 32, xr = L_X + fo * 16 + (ln & 7) * 2;
  unsigned oa = fo * 16 + (ln & 7) * 2, gl = L_H + (wl & 3) * H_S + ((ln >> 2) & 15) * 4;
  asm volatile("" : "+v"(oo), "+v"(ow), "+v"(fo_), "+v"(fh), "+v"(psg), "+v"(so), "+v"(frw), "+v"(frr), "+v"(xr), "+v"(oa), "+v"(gl), "+s"(obase), "+s"(sgb));
  if (w < 4) {
    for (unsigned spin = 0; any_bad((hv.y ^ tag) | (hv.w ^ tag)) && spin < SPIN_CAP; ++spin) {
      GATHER1(hv, go, hb);
    }
    float y0 = __uint_as_float(hv.x), y1 = __uint_as_float(hv.z);
    y0 = rbf(y0 + dpp<XOR1>(y0)); y1 = rbf(y1 + dpp<XOR1>(y1));
    const float u0_ = dpp<XOR2>(y0), u1_ = dpp<XOR2>(y1);
    const unsigned hh = pk(rbf(silu(y0)) * u0_, rbf(silu(y1)) * u1_);
    if ((lane & 3) == 0) *(LAS unsigned*)(L + gl) = hh;
    SLEEP(OSLEEP - 6);
  } else {
    SLEEP(OSLEEP - 3);
  }

  {
    v4u ov;
    for (unsigned spin = 0;; ++spin) {
      GATHER1(ov, oo, obase);
      if (!any_bad(((ov.y ^ ep) | (ov.w ^ ep)) >> 16) || spin >= SPIN_CAP) break;
    }
    unsigned r0, r1;
    float lv, mx, a0, a1, a2, a3;
    asm volatile(
        "v_perm_b32 %2, %9, %11, %12\n\t"
        "v_lshlrev_b32 %4, 16, %8\n\t"
        "v_and_b32 %5, 0xffff0000, %8\n\t"
        "v_max_f32_dpp %3, %2, %2 quad_perm:[1,0,3,2] row_mask:0xf bank_mask:0xf\n\t"
        "v_lshlrev_b32 %6, 16, %10\n\t"
        "v_and_b32 %7, 0xffff0000, %10\n\t"
        "v_max_f32_dpp %3, %3, %3 quad_perm:[2,3,0,1] row_mask:0xf bank_mask:0xf\n\t"
        "s_nop 1\n\t"
        "v_max_f32_dpp %3, %3, %3 row_half_mirror row_mask:0xf bank_mask:0xf\n\t"
        "v_sub_f32 %2, %2, %3\n\t"
        "v_exp_f32 %2, %2\n\t"
        "s_nop 0\n\t"
        "v_mul_f32 %4, %4, %2\n\t"
        "v_mul_f32 %5, %5, %2\n\t"
        "v_mul_f32 %6, %6, %2\n\t"
        "v_mul_f32 %7, %7, %2\n\t"
        "v_add_f32_dpp %2, %2, %2 quad_perm:[1,0,3,2] row_mask:0xf bank_mask:0xf\n\t"
        "v_add_f32_dpp %4, %4, %4 quad_perm:[1,0,3,2] row_mask:0xf bank_mask:0xf\n\t"
        "v_add_f32_dpp %5, %5, %5 quad_perm:[1,0,3,2] row_mask:0xf bank_mask:0xf\n\t"
        "v_add_f32_dpp %6, %6, %6 quad_perm:[1,0,3,2] row_mask:0xf bank_mask:0xf\n\t"
        "v_add_f32_dpp %7, %7, %7 quad_perm:[1,0,3,2] row_mask:0xf bank_mask:0xf\n\t"
        "v_add_f32_dpp %2, %2, %2 quad_perm:[2,3,0,1] row_mask:0xf bank_mask:0xf\n\t"
        "v_add_f32_dpp %4, %4, %4 quad_perm:[2,3,0,1] row_mask:0xf bank_mask:0xf\n\t"
        "v_add_f32_dpp %5, %5, %5 quad_perm:[2,3,0,1] row_mask:0xf bank_mask:0xf\n\t"
        "v_add_f32_dpp %6, %6, %6 quad_perm:[2,3,0,1] row_mask:0xf bank_mask:0xf\n\t"
        "v_add_f32_dpp %7, %7, %7 quad_perm:[2,3,0,1] row_mask:0xf bank_mask:0xf\n\t"
        "v_add_f32_dpp %2, %2, %2 row_half_mirror row_mask:0xf bank_mask:0xf\n\t"
        "v_add_f32_dpp %4, %4, %4 row_half_mirror row_mask:0xf bank_mask:0xf\n\t"
        "v_add_f32_dpp %5, %5, %5 row_half_mirror row_mask:0xf bank_mask:0xf\n\t"
        "v_rcp_f32 %2, %2\n\t"
        "v_add_f32_dpp %6, %6, %6 row_half_mirror row_mask:0xf bank_mask:0xf\n\t"
        "v_add_f32_dpp %7, %7, %7 row_half_mirror row_mask:0xf bank_mask:0xf\n\t"
        "v_mul_f32 %4, %4, %2\n\t"
        "v_mul_f32 %5, %5, %2\n\t"
        "v_mul_f32 %6, %6, %2\n\t"
        "v_mul_f32 %7, %7, %2\n\t"
        "v_cvt_pk_bf16_f32 %0, %4, %5\n\t"
        "v_cvt_pk_bf16_f32 %1, %6, %7"
        : "=&v"(r0), "=&v"(r1), "=&v"(lv), "=&v"(mx), "=&v"(a0), "=&v"(a1), "=&v"(a2), "=&v"(a3)
        : "v"(ov.x), "v"(ov.y), "v"(ov.z), "v"(ov.w), "s"(0x05040100u));
    if (os == 0) *(LAS v2u*)(L + ow) = v2u{r0, r1};
  }
  BAR();
  {
    const v4u b0 = lds128(L, fo_), b1 = lds128(L, fo_ + 64), b2 = lds128(L, fh);
    const v4u a0 = trfrag(L, tbv + (L_WO - L_V), 1024), a1 = trfrag(L, tbv + (L_WO - L_V) + 8192, 1024), a2 = trfrag(L, tbv + (L_W2 - L_V), 1024);
    SB();
    f32x4 s0 = {0.f, 0.f, 0.f, 0.f}, s1 = {0.f, 0.f, 0.f, 0.f}, g0 = {0.f, 0.f, 0.f, 0.f};
    s0 = mfma(a0, b0, s0);
    s1 = mfma(a1, b1, s1);
    g0 = mfma(a2, b2, g0);
    const float gv = has2 ? sel4(g0, uu) : 0.f;
    st_pair(ws, psg, v4u{__float_as_uint(sel4(s0 + s1, uu)), tag, __float_as_uint(gv), tag});
  }

  {
    v4u sv;
    VMWAIT(0);
    for (unsigned spin = 0;; ++spin) {
      GATHER1(sv, so, sgb);
      if (!any_bad((sv.y ^ tag) | (sv.w ^ tag)) || spin >= SPIN_CAP) break;
    }
    float s = __uint_as_float(sv.x), gg = __uint_as_float(sv.z);
    s += dpp<ROR8>(s); gg += dpp<ROR8>(gg);
    s = x16_sum(s); gg = x16_sum(gg);
    s = x32_sum(s); gg = x32_sum(gg);
    if (lane < 8) {
      *(LAS float*)(L + frw) = s;
      *(LAS float*)(L + frw + 256) = gg;
    }
  }
  BAR();
  if (w == 0) {
    const f32x4 a = *(LAS f32x4*)(L + frr), d = *(LAS f32x4*)(L + frr + 16);
    const float xv = lo16(*(LAS u16*)(L + xr));
    SB();
    const float t = rbf(((a[0] + a[1]) + (a[2] + a[3])) + ((d[0] + d[1]) + (d[2] + d[3])));
    const float gs = dpp<ROR8>(rbf(silu(t)));
    const float r = xv + rbf(t * gs);
    if (lane < 8) *(GAS u16*)((GAS unsigned char*)out + oa) = (u16)pk(r, r);
    if (c == 0 && lane == 0) __hip_atomic_store((gu32*)(ws + WS_E), tag, __ATOMIC_RELAXED, __HIP_MEMORY_SCOPE_AGENT);
  }
  asm volatile(".Lfd_end:");
}

extern "C" void kernel_launch(void* const* d_in, const int* in_sizes, int n_in, void* d_out, int out_size, void* d_ws, size_t ws_size,
                              hipStream_t stream) {
  (void)in_sizes; (void)n_in; (void)out_size; (void)ws_size;
  fused_decode_kernel<<<256, 512, 0, stream>>>((const u16*)d_in[0], (const u16*)d_in[1], (const u16*)d_in[2], (const u16*)d_in[3],
                                               (const u16*)d_in[4], (const u16*)d_in[6], (const u16*)d_in[7], (const u16*)d_in[8],
                                               (u16*)d_out, (unsigned char*)d_ws);
}
```
